```python
import torch

def _softmax(x, axis=-1):
    return torch.softmax(x, dim=axis)
def _gelu(x):
    return torch.nn.functional.gelu(x, approximate="tanh")
def _silu(x):
    return torch.nn.functional.silu(x)
def _sigmoid(x):
    return torch.sigmoid(x)
def _layernorm(x, axis=-1, eps=1e-5):
    m = x.mean(dim=axis, keepdim=True)
    v = x.var(dim=axis, keepdim=True, unbiased=False)
    return (x - m) / torch.sqrt(v + eps)
def _scatter_add(tgt, idx, src, axis=0):
    return tgt.clone().index_add_(axis, idx, src)
def _segment_sum(src, seg, n):
    out = torch.zeros((n,) + src.shape[1:], dtype=src.dtype)
    return out.index_add_(0, seg, src)
def _pad(x, pw):
    flat = tuple(v for pair in reversed(pw) for v in pair)
    return torch.nn.functional.pad(x, flat)

kernel_name = 'standalone__gqa_block_cached__decode'

def setup_inputs():

    _w = (lambda a: (lambda t: t.to(torch.bfloat16) if t.is_floating_point() else t)(torch.as_tensor(a)))
    B, Sq, D = 8, 1, 640
    Hq, Hkv, Sk, Dh = 32, 32, 4096, 192
    assert Hq % Hkv == 0, (Hq, Hkv)
    return {
        "x": _w((lambda *s: torch.randn(*s, dtype=torch.bfloat16) * 0.8)(B, Sq, D)),
        "k_cache": _w((lambda *s: torch.randn(*s, dtype=torch.bfloat16) * 0.8)(B, Hkv, Sk, Dh)),
        "v_cache": _w((lambda *s: torch.randn(*s, dtype=torch.bfloat16) * 0.05)(B, Hkv, Sk, Dh)),
        "wq": _w((lambda *s: torch.randn(*s, dtype=torch.bfloat16) * 0.8)(D, Hq * Dh) * (D ** -0.5)),
        "wo": _w((lambda *s: torch.randn(*s, dtype=torch.bfloat16) * 0.05)(Hq * Dh, D) * ((Hq * Dh) ** -0.5)),
    }

def reference(x, k_cache, v_cache, wq, wo):
    B, Sq, D = x.shape
    Hq, Dh = 32, 192
    Hkv = k_cache.shape[1]
    rep = Hq // Hkv
    q = (lambda x, *d: x.permute(*d))((x @ wq).reshape(B, Sq, Hq, Dh), 0, 2, 1, 3)
    k = (lambda x, n, axis: torch.repeat_interleave(x, n, dim=axis))(k_cache, rep, axis=1)
    v = (lambda x, n, axis: torch.repeat_interleave(x, n, dim=axis))(v_cache, rep, axis=1)
    scores = torch.einsum("bhqd,bhkd->bhqk", q, k) * (Dh ** -0.5)
    p = _softmax(scores, axis=-1)
    o = torch.einsum("bhqk,bhkd->bhqd", p, v)
    return (lambda x, *d: x.permute(*d))(o, 0, 2, 1, 3).reshape(B, Sq, Hq * Dh) @ wo
```

```cpp
#include <hip/hip_runtime.h>
#include <hip/hip_bf16.h>
#include <stdint.h>

constexpr int B = 8, D = 640, H = 32, SK = 4096, DH = 192, N = H * DH;
using u32x4 = __attribute__((ext_vector_type(4))) unsigned;
using bf16x2 = __attribute__((ext_vector_type(2))) __bf16;
typedef unsigned short u16;

__device__ __forceinline__ float bf2f(unsigned h) { return __builtin_bit_cast(float, h << 16); }
__device__ __forceinline__ float lo(unsigned w) { return __builtin_bit_cast(float, w << 16); }
__device__ __forceinline__ float hi(unsigned w) { return __builtin_bit_cast(float, w & 0xffff0000u); }
__device__ __forceinline__ unsigned cvtpk(float a, float b) {
  unsigned r; asm volatile("v_cvt_pk_bf16_f32 %0, %1, %2" : "=v"(r) : "v"(a), "v"(b)); return r;
}
__device__ __forceinline__ float dot2(unsigned k, unsigned q, float acc) {
  return __builtin_amdgcn_fdot2_f32_bf16(__builtin_bit_cast(bf16x2, k), __builtin_bit_cast(bf16x2, q), acc, false);
}
template <int CTRL> __device__ __forceinline__ float dpp(float x) {
  return __builtin_bit_cast(float, __builtin_amdgcn_mov_dpp(__builtin_bit_cast(int, x), CTRL, 0xf, 0xf, true));
}

__global__ void __launch_bounds__(256) qproj(const u16* __restrict__ x, const u16* __restrict__ wq, u16* __restrict__ q) {
  __shared__ float xs[B * D];
  __shared__ float red[32][B][64];
  const int tid = threadIdx.x;
  for (int i = tid; i < B * D; i += 256) xs[i] = bf2f(x[i]);
  __syncthreads();
  const int cc = tid & 7, kg = tid >> 3;
  const int n0 = blockIdx.x * 64 + cc * 8;
  float acc[B][8];
#pragma unroll
  for (int b = 0; b < B; ++b)
#pragma unroll
    for (int j = 0; j < 8; ++j) acc[b][j] = 0.f;
  u32x4 w[20];
#pragma unroll
  for (int r = 0; r < 20; ++r) w[r] = *reinterpret_cast<const u32x4*>(wq + (size_t)(kg * 20 + r) * N + n0);
#pragma unroll
  for (int r = 0; r < 20; ++r) {
    float wf[8];
#pragma unroll
    for (int k = 0; k < 4; ++k) { wf[2 * k] = lo(w[r][k]); wf[2 * k + 1] = hi(w[r][k]); }
#pragma unroll
    for (int b = 0; b < B; ++b) { const float xv = xs[b * D + kg * 20 + r];
#pragma unroll
      for (int j = 0; j < 8; ++j) acc[b][j] = fmaf(xv, wf[j], acc[b][j]); }
  }
#pragma unroll
  for (int b = 0; b < B; ++b)
#pragma unroll
    for (int j = 0; j < 8; ++j) red[kg][b][cc * 8 + j] = acc[b][j];
  __syncthreads();
  for (int i = tid; i < B * 64; i += 256) {
    const int b = i >> 6, c = i & 63; float s = 0.f;
    for (int g = 0; g < 32; ++g) s += red[g][b][c];
    q[b * N + blockIdx.x * 64 + c] = (u16)(cvtpk(s, s) & 0xffff);
  }
}

constexpr int NW = 8;
__global__ void __launch_bounds__(512) attn(const u16* __restrict__ q, const u16* __restrict__ kc, const u16* __restrict__ vc, u16* __restrict__ o) {
  __shared__ float sc[SK];
  __shared__ float red[NW * 8][DH];
  __shared__ float wred[2 * NW];
  const int bh = blockIdx.x, b = bh >> 5, h = bh & 31;
  const int tid = threadIdx.x, lane = tid & 63, wave = tid >> 6;
  const int g = lane >> 3, c = lane & 7;
  u32x4 qv[3];
#pragma unroll
  for (int i = 0; i < 3; ++i) qv[i] = *reinterpret_cast<const u32x4*>(q + b * N + h * DH + i * 64 + c * 8);
  const u16* kb = kc + (size_t)bh * SK * DH;
  const u16* vb = vc + (size_t)bh * SK * DH;
  for (int it = 0; it < 64; it += 4) {
    u32x4 kk[4][3];
#pragma unroll
    for (int u = 0; u < 4; ++u)
#pragma unroll
      for (int i = 0; i < 3; ++i)
        kk[u][i] = __builtin_nontemporal_load(reinterpret_cast<const u32x4*>(kb + (size_t)(wave * 512 + (it + u) * 8 + g) * DH + i * 64 + c * 8));
#pragma unroll
    for (int u = 0; u < 4; ++u) {
      float d = 0.f;
#pragma unroll
      for (int i = 0; i < 3; ++i)
#pragma unroll
        for (int k = 0; k < 4; ++k) d = dot2(kk[u][i][k], qv[i][k], d);
      d += dpp<0xB1>(d); d += dpp<0x4E>(d); d += dpp<0x141>(d);
      if (c == 0) sc[wave * 512 + (it + u) * 8 + g] = d * 0.07216878364870322f;
    }
  }
  __syncthreads();
  float m = -INFINITY;
  for (int i = tid; i < SK; i += 512) m = fmaxf(m, sc[i]);
  for (int s = 32; s; s >>= 1) m = fmaxf(m, __shfl_xor(m, s));
  if (lane == 0) wred[wave] = m;
  __syncthreads();
  m = wred[0];
  for (int w = 1; w < NW; ++w) m = fmaxf(m, wred[w]);
  float l = 0.f;
  for (int i = tid; i < SK; i += 512) { const float p = __expf(sc[i] - m); sc[i] = p; l += p; }
  for (int s = 32; s; s >>= 1) l += __shfl_xor(l, s);
  if (lane == 0) wred[NW + wave] = l;
  __syncthreads();
  l = 0.f;
  for (int w = 0; w < NW; ++w) l += wred[NW + w];
  float acc[24];
#pragma unroll
  for (int j = 0; j < 24; ++j) acc[j] = 0.f;
  for (int it = 0; it < 64; it += 4) {
    u32x4 vv[4][3];
#pragma unroll
    for (int u = 0; u < 4; ++u)
#pragma unroll
      for (int i = 0; i < 3; ++i)
        vv[u][i] = __builtin_nontemporal_load(reinterpret_cast<const u32x4*>(vb + (size_t)(wave * 512 + (it + u) * 8 + g) * DH + i * 64 + c * 8));
#pragma unroll
    for (int u = 0; u < 4; ++u) {
      const float p = sc[wave * 512 + (it + u) * 8 + g];
#pragma unroll
      for (int i = 0; i < 3; ++i)
#pragma unroll
        for (int k = 0; k < 4; ++k) { acc[i * 8 + 2 * k] = fmaf(p, lo(vv[u][i][k]), acc[i * 8 + 2 * k]); acc[i * 8 + 2 * k + 1] = fmaf(p, hi(vv[u][i][k]), acc[i * 8 + 2 * k + 1]); }
    }
  }
#pragma unroll
  for (int i = 0; i < 3; ++i)
#pragma unroll
    for (int j = 0; j < 8; ++j) red[wave * 8 + g][i * 64 + c * 8 + j] = acc[i * 8 + j];
  __syncthreads();
  if (tid < DH) {
    float s = 0.f;
    for (int r = 0; r < NW * 8; ++r) s += red[r][tid];
    s /= l;
    o[b * N + h * DH + tid] = (u16)(cvtpk(s, s) & 0xffff);
  }
}

__global__ void __launch_bounds__(256) oproj1(const u16* __restrict__ o, const u16* __restrict__ wo, float* __restrict__ part) {
  __shared__ float os[B][DH];
  __shared__ float red[24][B][80];
  const int cg = blockIdx.x & 7, h = blockIdx.x >> 3, tid = threadIdx.x;
  for (int i = tid; i < B * DH; i += 256) { const int b = i / DH, d = i % DH; os[b][d] = bf2f(o[b * N + h * DH + d]); }
  __syncthreads();
  if (tid < 240) {
    const int cc = tid % 10, rg = tid / 10;
    float acc[B][8];
#pragma unroll
    for (int b = 0; b < B; ++b)
#pragma unroll
      for (int j = 0; j < 8; ++j) acc[b][j] = 0.f;
    u32x4 w[8];
#pragma unroll
    for (int r = 0; r < 8; ++r) w[r] = *reinterpret_cast<const u32x4*>(wo + (size_t)(h * DH + rg * 8 + r) * D + cg * 80 + cc * 8);
#pragma unroll
    for (int r = 0; r < 8; ++r) {
      float wf[8];
#pragma unroll
      for (int k = 0; k < 4; ++k) { wf[2 * k] = lo(w[r][k]); wf[2 * k + 1] = hi(w[r][k]); }
#pragma unroll
      for (int b = 0; b < B; ++b) { const float ov = os[b][rg * 8 + r];
#pragma unroll
        for (int j = 0; j < 8; ++j) acc[b][j] = fmaf(ov, wf[j], acc[b][j]); }
    }
#pragma unroll
    for (int b = 0; b < B; ++b)
#pragma unroll
      for (int j = 0; j < 8; ++j) red[rg][b][cc * 8 + j] = acc[b][j];
  }
  __syncthreads();
  for (int i = tid; i < B * 80; i += 256) {
    const int b = i / 80, c = i % 80; float s = 0.f;
    for (int g = 0; g < 24; ++g) s += red[g][b][c];
    part[(size_t)h * (B * D) + b * D + cg * 80 + c] = s;
  }
}
__global__ void __launch_bounds__(256) oproj2(const float* __restrict__ part, u16* __restrict__ out) {
  const int i = blockIdx.x * 256 + threadIdx.x;
  float s = 0.f;
#pragma unroll
  for (int h = 0; h < H; ++h) s += part[(size_t)h * (B * D) + i];
  out[i] = (u16)(cvtpk(s, s) & 0xffff);
}

extern "C" void kernel_launch(void* const* d_in, const int* in_sizes, int n_in, void* d_out, int out_size, void* d_ws, size_t ws_size, hipStream_t stream) {
  const u16* x = (const u16*)d_in[0]; const u16* kc = (const u16*)d_in[1]; const u16* vc = (const u16*)d_in[2];
  const u16* wq = (const u16*)d_in[3]; const u16* wo = (const u16*)d_in[4];
  u16* q = (u16*)d_ws; u16* o = q + B * N; float* part = (float*)((char*)d_ws + 2 * B * N * 2);
  qproj<<<96, 256, 0, stream>>>(x, wq, q);
  attn<<<256, 512, 0, stream>>>(q, kc, vc, o);
  oproj1<<<256, 256, 0, stream>>>(o, wo, part);
  oproj2<<<B * D / 256, 256, 0, stream>>>(part, (u16*)d_out);
}
```

```cpp
#include <hip/hip_runtime.h>
#include <hip/hip_bf16.h>
#include <stdint.h>

namespace {
constexpr int B = 8, D = 640, H = 32, SK = 4096, DH = 192, N = H * DH;
constexpr int NW = 8, NTHR = NW * 64;
constexpr unsigned HEADB = SK * DH * 2;
constexpr float C2 = 0.07216878364870322f * 1.4426950408889634f;

typedef unsigned short u16;
typedef unsigned long long u64;
using u32x4 = __attribute__((ext_vector_type(4))) unsigned;
using f32x4 = __attribute__((ext_vector_type(4))) float;
using bf16x2 = __attribute__((ext_vector_type(2))) __bf16;
typedef __attribute__((address_space(1))) u64 gu64;
#define RLX_AGENT __ATOMIC_RELAXED, __HIP_MEMORY_SCOPE_AGENT

constexpr int WS_EPOCH = 0, WS_SEEN = 64, WS_GRAN = 1024;
constexpr int WS_STAMP = 1 << 20;
constexpr u64 TAG_MASK = (1ull << 40) - 1;
constexpr unsigned SPIN_CAP = 1u << 22;

#define WQ_NT 0
#define WO_NT 0
#define PRO_BAR 1
#define WQ_ROT 0
#define ROT_MUL 7

__device__ __forceinline__ float lo16(unsigned w) { return __builtin_bit_cast(float, w << 16); }
__device__ __forceinline__ float hi16(unsigned w) { return __builtin_bit_cast(float, w & 0xffff0000u); }
__device__ __forceinline__ unsigned cvtpk(float a, float b) {
  unsigned r; asm("v_cvt_pk_bf16_f32 %0, %1, %2" : "=v"(r) : "v"(a), "v"(b)); return r;
}
__device__ __forceinline__ float dot2(unsigned k, unsigned q, float acc) {
  return __builtin_amdgcn_fdot2_f32_bf16(__builtin_bit_cast(bf16x2, k), __builtin_bit_cast(bf16x2, q), acc, false);
}
template <int CTRL> __device__ __forceinline__ float dpp(float x) {
  return __builtin_bit_cast(float, __builtin_amdgcn_mov_dpp(__builtin_bit_cast(int, x), CTRL, 0xf, 0xf, true));
}
constexpr int XOR1 = 0xB1, XOR2 = 0x4E, HMIR = 0x141, ROR8 = 0x128, ROR4 = 0x124;
__device__ __forceinline__ float swapadd32(float x, float y) {
  auto s = __builtin_amdgcn_permlane32_swap(__float_as_uint(x), __float_as_uint(y), false, false);
  return __uint_as_float(s[0]) + __uint_as_float(s[1]);
}
__device__ __forceinline__ float swapadd16(float x, float y) {
  auto s = __builtin_amdgcn_permlane16_swap(__float_as_uint(x), __float_as_uint(y), false, false);
  return __uint_as_float(s[0]) + __uint_as_float(s[1]);
}
__device__ __forceinline__ float wave_max(float x) {
  x = fmaxf(x, dpp<XOR1>(x)); x = fmaxf(x, dpp<XOR2>(x)); x = fmaxf(x, dpp<HMIR>(x)); x = fmaxf(x, dpp<ROR8>(x));
  auto s = __builtin_amdgcn_permlane16_swap(__float_as_uint(x), __float_as_uint(x), false, false);
  x = fmaxf(__uint_as_float(s[0]), __uint_as_float(s[1]));
  auto t = __builtin_amdgcn_permlane32_swap(__float_as_uint(x), __float_as_uint(x), false, false);
  return fmaxf(__uint_as_float(t[0]), __uint_as_float(t[1]));
}
__device__ __forceinline__ float wave_sum(float x) {
  x += dpp<XOR1>(x); x += dpp<XOR2>(x); x += dpp<HMIR>(x); x += dpp<ROR8>(x);
  x = swapadd16(x, x);
  return swapadd32(x, x);
}
__device__ __forceinline__ void reduce24(const float (&acc)[24], float (&r)[3], int lane) {
  float r1[12], r2[6];
#pragma unroll
  for (int a = 0; a < 12; ++a) r1[a] = swapadd32(acc[a], acc[a + 12]);
#pragma unroll
  for (int a = 0; a < 6; ++a) r2[a] = swapadd16(r1[a], r1[a + 6]);
#pragma unroll
  for (int a = 0; a < 3; ++a) { const float t1 = r2[a] + dpp<ROR8>(r2[a]), t2 = r2[a + 3] + dpp<ROR8>(r2[a + 3]); r[a] = (lane & 8) ? t2 : t1; }
}
__device__ __forceinline__ u32x4 bload(__amdgpu_buffer_rsrc_t rs, unsigned voff, unsigned soff, int aux_nt) {
  return aux_nt ? __builtin_amdgcn_raw_buffer_load_b128(rs, voff, soff, 2) : __builtin_amdgcn_raw_buffer_load_b128(rs, voff, soff, 0);
}
__device__ __forceinline__ void fma24(float (&acc)[24], const u32x4* w, float p) {
#pragma unroll
  for (int i = 0; i < 3; ++i)
#pragma unroll
    for (int k = 0; k < 4; ++k) {
      acc[i * 8 + 2 * k] = fmaf(p, lo16(w[i][k]), acc[i * 8 + 2 * k]);
      acc[i * 8 + 2 * k + 1] = fmaf(p, hi16(w[i][k]), acc[i * 8 + 2 * k + 1]);
    }
}
#define SB() __builtin_amdgcn_sched_barrier(0)
#define PIN1(a) asm volatile("" : "+v"(a) :: "memory")
#define PIN8(a, o) asm volatile("" : "+v"(a[o]), "+v"(a[o + 1]), "+v"(a[o + 2]), "+v"(a[o + 3]), "+v"(a[o + 4]), "+v"(a[o + 5]), "+v"(a[o + 6]), "+v"(a[o + 7]) :: "memory")
#define PIN24(a) do { PIN8(a, 0); PIN8(a, 8); PIN8(a, 16); } while (0)
#define LDS_FENCE() asm volatile("s_waitcnt lgkmcnt(0)" ::: "memory")
__device__ __forceinline__ void wg_barrier() {
  asm volatile("s_waitcnt lgkmcnt(0)\n\ts_barrier" ::: "memory");
}
#define STAMP(i) do { } while (0)

constexpr int L_P = 0;
constexpr int L_RED = 6144;
constexpr int L_ML = L_RED + NW * 192;
constexpr int L_Q = L_ML + 2 * NW;
constexpr int L_T = L_Q + 96;
constexpr int L_END = L_T + 4;

__global__ void __launch_bounds__(NTHR, 2)
attn_fused(const u16* __restrict__ x, const char* __restrict__ kc, const char* __restrict__ vc, const char* __restrict__ wq,
           const char* __restrict__ wo, u16* __restrict__ out, u64* __restrict__ ws_) {
  __shared__ __attribute__((aligned(16))) float lds[L_END];
  gu64* ws = (gu64*)ws_;
  const int tid = threadIdx.x, lane = tid & 63, wave = __builtin_amdgcn_readfirstlane(tid >> 6);
  const int bid = blockIdx.x, h = (bid & 7) * 4 + ((bid >> 3) & 3), b = bid >> 5, bh = b * H + h;
  const int g = lane >> 3, c = lane & 7;
  STAMP(0);
  const __amdgpu_buffer_rsrc_t rsK = __builtin_amdgcn_make_buffer_rsrc((void*)(kc + (size_t)bh * HEADB), 0, HEADB, 0x00020000);
  const __amdgpu_buffer_rsrc_t rsV = __builtin_amdgcn_make_buffer_rsrc((void*)(vc + (size_t)bh * HEADB), 0, HEADB, 0x00020000);
  const __amdgpu_buffer_rsrc_t rsQ = __builtin_amdgcn_make_buffer_rsrc((void*)(wq + h * (DH * 2)), 0, D * N * 2 - h * (DH * 2), 0x00020000);
  const __amdgpu_buffer_rsrc_t rsO = __builtin_amdgcn_make_buffer_rsrc((void*)(wo + (size_t)h * DH * D * 2), 0, DH * D * 2, 0x00020000);

  const u64 E = __hip_atomic_load(ws + WS_EPOCH, RLX_AGENT);
  const int wq_w = WQ_ROT ? ((wave + b) & 7) : wave;
  unsigned xw[5];
  {
    const unsigned* xp = reinterpret_cast<const unsigned*>(x + b * D + wq_w * 80 + g * 10);
#pragma unroll
    for (int i = 0; i < 5; ++i) xw[i] = xp[i];
  }
  u32x4 wt[30];
  {
    const unsigned voff = (unsigned)(wq_w * 80 + g * 10) * (N * 2) + c * 16;
#pragma unroll
    for (int i = 0; i < 10; ++i)
#pragma unroll
      for (int t = 0; t < 3; ++t) wt[i * 3 + t] = bload(rsQ, voff + t * 128, i * (N * 2), WQ_NT);
  }
  SB();
  STAMP(7);
  asm volatile("s_barrier" ::: "memory");
  const unsigned kvoff = g * 384 + c * 16;
  const int rot = (bid * ROT_MUL) & 63;
#define GOFF(n) ((unsigned)((((n) + rot) & 63) * 8 + wave) * 3072u)
  u32x4 buf[24];
#pragma unroll
  for (int u = 0; u < 8; ++u)
#pragma unroll
    for (int t = 0; t < 3; ++t) buf[u * 3 + t] = bload(rsK, kvoff + t * 128, GOFF(u), 1);
  SB();

  float acc[24];
#pragma unroll
  for (int j = 0; j < 24; ++j) acc[j] = 0.f;
#pragma unroll
  for (int i = 0; i < 10; ++i) { fma24(acc, &wt[i * 3], (i & 1) ? hi16(xw[i >> 1]) : lo16(xw[i >> 1])); PIN24(acc); SB(); }
  {
    float r[3]; reduce24(acc, r, lane);
#pragma unroll
    for (int a = 0; a < 3; ++a) lds[L_RED + wave * 192 + a * 64 + lane] = r[a];
  }
  if (wave == 0 && lane == 0) {
    const u64 T = E + 1;
    reinterpret_cast<u64*>(lds + L_T)[0] = T;
    __hip_atomic_store(ws + WS_SEEN + bid, T, RLX_AGENT);
  }
  STAMP(8);
  wg_barrier();
  if (tid < 192) {
    float s = 0.f;
#pragma unroll
    for (int w = 0; w < NW; ++w) s += lds[L_RED + w * 192 + tid];
    const int A = 3 * g + wave;
    const int d = (A >> 3) * 64 + c * 8 + (A & 7);
    reinterpret_cast<u16*>(lds + L_Q)[d] = (u16)cvtpk(s, s);
  }
  wg_barrier();
  u32x4 qv[3];
#pragma unroll
  for (int t = 0; t < 3; ++t) qv[t] = *reinterpret_cast<const u32x4*>(reinterpret_cast<const char*>(lds + L_Q) + t * 128 + c * 16);
  STAMP(1);

  float* pw = lds + L_P + wave * 512;
  auto kbatch = [&](int bt, auto last) {
    float xs[8];
#pragma unroll
    for (int u = 0; u < 8; ++u) {
      float d = 0.f;
#pragma unroll
      for (int t = 0; t < 3; ++t)
#pragma unroll
        for (int k = 0; k < 4; ++k) d = dot2(buf[u * 3 + t][k], qv[t][k], d);
      PIN1(d);
      xs[u] = d;
      SB();
#pragma unroll
      for (int t = 0; t < 3; ++t)
        buf[u * 3 + t] = last ? bload(rsV, kvoff + t * 128, GOFF(u), 1) : bload(rsK, kvoff + t * 128, GOFF(bt * 8 + 8 + u), 1);
      SB();
    }
    float y[4], z[2];
#pragma unroll
    for (int j = 0; j < 4; ++j) { const float t1 = xs[j] + dpp<HMIR>(xs[j]), t2 = xs[j + 4] + dpp<HMIR>(xs[j + 4]); y[j] = (c & 4) ? t2 : t1; }
#pragma unroll
    for (int j = 0; j < 2; ++j) { const float t1 = y[j] + dpp<XOR2>(y[j]), t2 = y[j + 2] + dpp<XOR2>(y[j + 2]); z[j] = (c & 2) ? t2 : t1; }
    const float t1 = z[0] + dpp<XOR1>(z[0]), t2 = z[1] + dpp<XOR1>(z[1]);
    pw[bt * 64 + lane] = (c & 1) ? t2 : t1;
  };
  for (int bt = 0; bt < 7; ++bt) kbatch(bt, std::false_type{});
  kbatch(7, std::true_type{});
  STAMP(2);

  float m, l;
  {
    float s[8];
    LDS_FENCE();
#pragma unroll
    for (int i = 0; i < 8; ++i) s[i] = pw[i * 64 + lane];
    m = s[0];
#pragma unroll
    for (int i = 1; i < 8; ++i) m = fmaxf(m, s[i]);
    m = wave_max(m);
    l = 0.f;
#pragma unroll
    for (int i = 0; i < 8; ++i) { const float p = __builtin_amdgcn_exp2f((s[i] - m) * C2); l += p; pw[i * 64 + lane] = p; }
    l = wave_sum(l);
    LDS_FENCE();
  }

#pragma unroll
  for (int j = 0; j < 24; ++j) acc[j] = 0.f;
  u32x4 wx[6];
  const unsigned ovoff = (unsigned)(wave * 24 + 2 * (lane >> 4)) * (D * 2) + (lane & 15) * 16;
#define OOFF(q) ((unsigned)(((q) / 10) * 8 + (((q) / 5) & 1)) * (D * 2) + ((q) % 5) * 256)
  auto vbatch = [&](int bt, auto last) {
    const f32x4 p0 = *reinterpret_cast<const f32x4*>(pw + bt * 64 + g * 8), p1 = *reinterpret_cast<const f32x4*>(pw + bt * 64 + g * 8 + 4);
    if (last) {
#pragma unroll
      for (int q = 0; q < 6; ++q) wx[q] = bload(rsO, ovoff, OOFF(24 + q), WO_NT);
      SB();
    }
#pragma unroll
    for (int u = 0; u < 8; ++u) {
      fma24(acc, &buf[u * 3], u < 4 ? p0[u & 3] : p1[u & 3]);
      PIN24(acc);
      SB();
#pragma unroll
      for (int t = 0; t < 3; ++t)
        buf[u * 3 + t] = last ? bload(rsO, ovoff, OOFF(u * 3 + t), WO_NT) : bload(rsV, kvoff + t * 128, GOFF(bt * 8 + 8 + u), 1);
      SB();
    }
  };
  for (int bt = 0; bt < 7; ++bt) vbatch(bt, std::false_type{});
  vbatch(7, std::true_type{});
  STAMP(3);

  {
    float r[3]; reduce24(acc, r, lane);
#pragma unroll
    for (int a = 0; a < 3; ++a) lds[L_RED + wave * 192 + a * 64 + lane] = r[a];
    if (lane == 0) { lds[L_ML + 2 * wave] = m; lds[L_ML + 2 * wave + 1] = l; }
  }
  wg_barrier();
  if (tid < 192) {
    float M = lds[L_ML];
#pragma unroll
    for (int w = 1; w < NW; ++w) M = fmaxf(M, lds[L_ML + 2 * w]);
    float L = 0.f, s = 0.f;
#pragma unroll
    for (int w = 0; w < NW; ++w) {
      const float f = __builtin_amdgcn_exp2f((lds[L_ML + 2 * w] - M) * C2);
      L = fmaf(f, lds[L_ML + 2 * w + 1], L);
      s = fmaf(f, lds[L_RED + w * 192 + tid], s);
    }
    s = s / L;
    const int A = 3 * g + wave;
    const int d = (A >> 3) * 64 + c * 8 + (A & 7);
    reinterpret_cast<u16*>(lds + L_Q)[d] = (u16)cvtpk(s, s);
  }
  wg_barrier();
  STAMP(4);

  {
    float oa[40];
#pragma unroll
    for (int j = 0; j < 40; ++j) oa[j] = 0.f;
#pragma unroll
    for (int jj = 0; jj < 3; ++jj) {
      const unsigned op = reinterpret_cast<const unsigned*>(lds + L_Q)[wave * 12 + jj * 4 + (lane >> 4)];
#pragma unroll
      for (int i5 = 0; i5 < 5; ++i5) {
        const int q0 = jj * 10 + i5, q1 = q0 + 5;
        const u32x4 A = q0 < 24 ? buf[q0] : wx[q0 - 24], Bv = q1 < 24 ? buf[q1] : wx[q1 - 24];
#pragma unroll
        for (int k = 0; k < 4; ++k) {
          oa[i5 * 8 + 2 * k] = dot2(__builtin_amdgcn_perm(Bv[k], A[k], 0x05040100u), op, oa[i5 * 8 + 2 * k]);
          oa[i5 * 8 + 2 * k + 1] = dot2(__builtin_amdgcn_perm(Bv[k], A[k], 0x07060302u), op, oa[i5 * 8 + 2 * k + 1]);
        }
      }
    }
    float r1[20], r2[10];
#pragma unroll
    for (int a = 0; a < 20; ++a) r1[a] = swapadd32(oa[a], oa[a + 20]);
#pragma unroll
    for (int a = 0; a < 10; ++a) r2[a] = swapadd16(r1[a], r1[a + 10]);
    const int A0 = 10 * ((lane >> 4) & 1) + 20 * (lane >> 5), cc = lane & 15;
#pragma unroll
    for (int a = 0; a < 10; ++a) {
      const int A = A0 + a, col = (A >> 3) * 128 + cc * 8 + (A & 7);
      lds[L_P + wave * 720 + col + (col >> 3)] = r2[a];
    }
  }
  wg_barrier();
  const u64 T = reinterpret_cast<const u64*>(lds + L_T)[0];
  const u64 tag = T & TAG_MASK;
#pragma unroll
  for (int rep = 0; rep < 2; ++rep) {
    const int col = tid + rep * NTHR;
    if (col < D) {
      float s = 0.f;
#pragma unroll
      for (int w = 0; w < NW; ++w) s += lds[L_P + w * 720 + col + (col >> 3)];
      const unsigned bits = __float_as_uint(s) + 0x80u;
      const int cg = col / 20, cr = col - cg * 20;
      __hip_atomic_store(ws + WS_GRAN + ((b * 32 + cg) * 32 + h) * 20 + cr, ((u64)(bits >> 8) << 40) | tag, RLX_AGENT);
    }
  }
  STAMP(5);

  if (wave < 5) {
    gu64* gp = ws + WS_GRAN + (b * 32 + h) * 32 * 20 + (lane >> 2) * 20 + wave * 4 + (lane & 3);
    u64 g0, g1;
    for (unsigned spins = 0;; ++spins) {
      g0 = __hip_atomic_load(gp, RLX_AGENT);
      g1 = __hip_atomic_load(gp + 16 * 20, RLX_AGENT);
      const bool ok = (g0 & TAG_MASK) == tag && (g1 & TAG_MASK) == tag;
      if (__all(ok) || spins > SPIN_CAP) break;
    }
    float v = __uint_as_float((unsigned)(g0 >> 40) << 8) + __uint_as_float((unsigned)(g1 >> 40) << 8);
    v += dpp<ROR4>(v); v += dpp<ROR8>(v);
    v = swapadd16(v, v);
    v = swapadd32(v, v);
    if (lane < 4) out[b * D + h * 20 + wave * 4 + lane] = (u16)cvtpk(v, v);
  } else if (wave == 7 && bid == 0) {
    for (unsigned spins = 0;; ++spins) {
      bool ok = true;
#pragma unroll
      for (int i = 0; i < 4; ++i) ok &= __hip_atomic_load(ws + WS_SEEN + i * 64 + lane, RLX_AGENT) == T;
      if (__all(ok) || spins > SPIN_CAP) break;
    }
    if (lane == 0) __hip_atomic_store(ws + WS_EPOCH, T, RLX_AGENT);
  }
  STAMP(6);
}
}

extern "C" void kernel_launch(void* const* d_in, const int* in_sizes, int n_in, void* d_out, int out_size, void* d_ws, size_t ws_size,
                              hipStream_t stream) {
  attn_fused<<<256, NTHR, 0, stream>>>((const u16*)d_in[0], (const char*)d_in[1], (const char*)d_in[2], (const char*)d_in[3],
                                       (const char*)d_in[4], (u16*)d_out, (u64*)d_ws);
}
```

```cpp
#include <hip/hip_runtime.h>
#include <hip/hip_bf16.h>
#include <stdint.h>

namespace {
constexpr int B = 8, D = 640, H = 32, SK = 4096, DH = 192, N = H * DH;
constexpr int NW = 8, NTHR = NW * 64;
constexpr unsigned HEADB = SK * DH * 2;
constexpr float C2 = 0.07216878364870322f * 1.4426950408889634f;

typedef unsigned short u16;
typedef unsigned long long u64;
using u32x4 = __attribute__((ext_vector_type(4))) unsigned;
using f32x4 = __attribute__((ext_vector_type(4))) float;
using u32x2 = __attribute__((ext_vector_type(2))) unsigned;
using f32x2 = __attribute__((ext_vector_type(2))) float;
using bf16x2 = __attribute__((ext_vector_type(2))) __bf16;
using bf16x8 = __attribute__((ext_vector_type(8))) __bf16;
using s16x4 = __attribute__((ext_vector_type(4))) short;
typedef __attribute__((address_space(3))) s16x4* lds_s16x4;
typedef __attribute__((address_space(1))) u64 gu64;
#define RLX_AGENT __ATOMIC_RELAXED, __HIP_MEMORY_SCOPE_AGENT

constexpr int WS_EPOCH = 0, WS_SEEN = 64, WS_GQ = 1024, WS_GO = WS_GQ + H * B * 8 * DH, WS_GF = WS_GO + H * B * 96;
constexpr int WS_STAMP = 1 << 20;
constexpr u64 TAG_MASK = (1ull << 40) - 1;
constexpr unsigned SPIN_CAP = 1u << 20;
#define W_AUX 2
#define X_AUX 0
#define ROT_MUL 7
#define PRO_BAR 1
#define INF 2
#define PACE_T 11900
#define PACE_C 1
#define POLL_SLEEP 1
__device__ __forceinline__ float lo16(unsigned w) { return __builtin_bit_cast(float, w << 16); }
__device__ __forceinline__ float hi16(unsigned w) { return __builtin_bit_cast(float, w & 0xffff0000u); }
__device__ __forceinline__ unsigned cvtpk(float a, float b) {
  unsigned r; asm("v_cvt_pk_bf16_f32 %0, %1, %2" : "=v"(r) : "v"(a), "v"(b)); return r;
}
__device__ __forceinline__ float dot2(unsigned k, unsigned q, float acc) {
  return __builtin_amdgcn_fdot2_f32_bf16(__builtin_bit_cast(bf16x2, k), __builtin_bit_cast(bf16x2, q), acc, false);
}
template <int CTRL> __device__ __forceinline__ float dpp(float x) {
  return __builtin_bit_cast(float, __builtin_amdgcn_mov_dpp(__builtin_bit_cast(int, x), CTRL, 0xf, 0xf, true));
}
constexpr int XOR1 = 0xB1, XOR2 = 0x4E, HMIR = 0x141, ROR8 = 0x128, ROR4 = 0x124;
__device__ __forceinline__ float swapadd32(float x, float y) {
  auto s = __builtin_amdgcn_permlane32_swap(__float_as_uint(x), __float_as_uint(y), false, false);
  return __uint_as_float(s[0]) + __uint_as_float(s[1]);
}
__device__ __forceinline__ float swapadd16(float x, float y) {
  auto s = __builtin_amdgcn_permlane16_swap(__float_as_uint(x), __float_as_uint(y), false, false);
  return __uint_as_float(s[0]) + __uint_as_float(s[1]);
}
__device__ __forceinline__ float wave_max(float x) {
  x = fmaxf(x, dpp<XOR1>(x)); x = fmaxf(x, dpp<XOR2>(x)); x = fmaxf(x, dpp<HMIR>(x)); x = fmaxf(x, dpp<ROR8>(x));
  auto s = __builtin_amdgcn_permlane16_swap(__float_as_uint(x), __float_as_uint(x), false, false);
  x = fmaxf(__uint_as_float(s[0]), __uint_as_float(s[1]));
  auto t = __builtin_amdgcn_permlane32_swap(__float_as_uint(x), __float_as_uint(x), false, false);
  return fmaxf(__uint_as_float(t[0]), __uint_as_float(t[1]));
}
__device__ __forceinline__ float wave_sum(float x) {
  x += dpp<XOR1>(x); x += dpp<XOR2>(x); x += dpp<HMIR>(x); x += dpp<ROR8>(x);
  x = swapadd16(x, x);
  return swapadd32(x, x);
}
__device__ __forceinline__ void reduce24(const float (&acc)[24], float (&r)[3], int lane) {
  float r1[12], r2[6];
#pragma unroll
  for (int a = 0; a < 12; ++a) r1[a] = swapadd32(acc[a], acc[a + 12]);
#pragma unroll
  for (int a = 0; a < 6; ++a) r2[a] = swapadd16(r1[a], r1[a + 6]);
#pragma unroll
  for (int a = 0; a < 3; ++a) { const float t1 = r2[a] + dpp<ROR8>(r2[a]), t2 = r2[a + 3] + dpp<ROR8>(r2[a + 3]); r[a] = (lane & 8) ? t2 : t1; }
}
__device__ __forceinline__ u32x4 bload(__amdgpu_buffer_rsrc_t rs, unsigned voff, unsigned soff, int aux_nt) {
  return aux_nt ? __builtin_amdgcn_raw_buffer_load_b128(rs, voff, soff, 2) : __builtin_amdgcn_raw_buffer_load_b128(rs, voff, soff, 0);
}
__device__ __forceinline__ void fma24(float (&acc)[24], const u32x4* w, float p) {
#pragma unroll
  for (int i = 0; i < 3; ++i)
#pragma unroll
    for (int k = 0; k < 4; ++k) {
      acc[i * 8 + 2 * k] = fmaf(p, lo16(w[i][k]), acc[i * 8 + 2 * k]);
      acc[i * 8 + 2 * k + 1] = fmaf(p, hi16(w[i][k]), acc[i * 8 + 2 * k + 1]);
    }
}
#define SB() __builtin_amdgcn_sched_barrier(0)
#define PIN1(a) asm volatile("" : "+v"(a) :: "memory")
#define PIN8(a, o) asm volatile("" : "+v"(a[o]), "+v"(a[o + 1]), "+v"(a[o + 2]), "+v"(a[o + 3]), "+v"(a[o + 4]), "+v"(a[o + 5]), "+v"(a[o + 6]), "+v"(a[o + 7]) :: "memory")
#define PIN24(a) do { PIN8(a, 0); PIN8(a, 8); PIN8(a, 16); } while (0)
#define LDS_FENCE() asm volatile("s_waitcnt lgkmcnt(0)" ::: "memory")
__device__ __forceinline__ void wg_barrier() {
  asm volatile("s_waitcnt lgkmcnt(0)\n\ts_barrier" ::: "memory");
}
#define STAMP(i) do { } while (0)

constexpr int QSTR = 144, OSTR = 176, ASTR = 416;
constexpr int L_P = 0;
constexpr int L_RED = L_P + NW * 512 * 4;
constexpr int L_ML = L_RED + NW * 192 * 4;
constexpr int L_Q = L_ML + 2 * NW * 4;
constexpr int L_T = L_Q + 384;
constexpr int L_OALL = L_T + 16;
constexpr int L_X = L_OALL + 8 * ASTR;
constexpr int L_WQ = L_X + 3 * 1280;
constexpr int L_WO = L_WQ + 3 * 80 * QSTR;
constexpr int L_END = L_WO + 256 * OSTR;

__device__ __forceinline__ s16x4 tr_read(const char* p) { return __builtin_amdgcn_ds_read_tr16_b64_v4i16((lds_s16x4)p); }
__device__ __forceinline__ u64 gran24(float v, u64 tag) { return ((u64)((__float_as_uint(v) + 0x80u) >> 8) << 40) | tag; }
__device__ __forceinline__ float pay24(unsigned hi) { return __uint_as_float(hi & 0xffffff00u); }
__device__ __forceinline__ bool tag40_ok(unsigned lo, unsigned hi, u64 tag) { return lo == (unsigned)tag && (hi & 0xffu) == (unsigned)(tag >> 32); }

__global__ void __launch_bounds__(NTHR, 2)
attn_fused(const u16* __restrict__ x, const char* __restrict__ kc, const char* __restrict__ vc, const char* __restrict__ wq,
           const char* __restrict__ wo, u16* __restrict__ out, u64* __restrict__ ws_) {
  __shared__ __attribute__((aligned(16))) char lds[L_END];
  gu64* ws = (gu64*)ws_;
  const int tid = threadIdx.x, lane = tid & 63, wave = __builtin_amdgcn_readfirstlane(tid >> 6);
  const int bid = blockIdx.x, h = (bid & 7) * 4 + ((bid >> 3) & 3), b = bid >> 5, bh = b * H + h;
  const int g = lane >> 3, c = lane & 7;
  STAMP(0);
  const u64 t_in = __builtin_amdgcn_s_memrealtime();
  const __amdgpu_buffer_rsrc_t rsK = __builtin_amdgcn_make_buffer_rsrc((void*)(kc + (size_t)bh * HEADB), 0, HEADB, 0x00020000);
  const __amdgpu_buffer_rsrc_t rsV = __builtin_amdgcn_make_buffer_rsrc((void*)(vc + (size_t)bh * HEADB), 0, HEADB, 0x00020000);
  const __amdgpu_buffer_rsrc_t rsW = __builtin_amdgcn_make_buffer_rsrc((void*)ws_, 0, (WS_GF + 8 * 32 * H * 20) * 8, 0x00020000);

  if (wave < 3) {
    const __amdgpu_buffer_rsrc_t rsQ = __builtin_amdgcn_make_buffer_rsrc((void*)(wq + h * (DH * 2)), 0, D * N * 2 - h * (DH * 2), 0x00020000);
    const __amdgpu_buffer_rsrc_t rsX = __builtin_amdgcn_make_buffer_rsrc((void*)x, 0, B * D * 2, 0x00020000);
    const u64 E = __hip_atomic_load(ws + WS_EPOCH, RLX_AGENT);
    const int c1 = 64 + (lane & 15);
    const u32x4 x0 = __builtin_amdgcn_raw_buffer_load_b128(rsX, (lane / 10) * (D * 2) + b * 160 + (lane % 10) * 16, 0, X_AUX);
    const u32x4 x1 = __builtin_amdgcn_raw_buffer_load_b128(rsX, (c1 / 10) * (D * 2) + b * 160 + (c1 % 10) * 16, 0, X_AUX);
    u32x4 wt[10];
    const unsigned voff = (unsigned)(b * 80 + g) * (N * 2) + wave * 128 + c * 16;
#pragma unroll
    for (int i = 0; i < 10; ++i) wt[i] = __builtin_amdgcn_raw_buffer_load_b128(rsQ, voff, i * 8 * (N * 2), W_AUX);
    SB();
    asm volatile("s_barrier" ::: "memory");
    STAMP(7);
    char* lx = lds + L_X + wave * 1280;
    char* lw = lds + L_WQ + wave * (80 * QSTR);
    *reinterpret_cast<u32x4*>(lx + lane * 16) = x0;
    *reinterpret_cast<u32x4*>(lx + c1 * 16) = x1;
#pragma unroll
    for (int i = 0; i < 10; ++i) *reinterpret_cast<u32x4*>(lw + (i * 8 + g) * QSTR + c * 16) = wt[i];
    f32x4 qa[4];
#pragma unroll
    for (int t = 0; t < 4; ++t) qa[t] = f32x4{0.f, 0.f, 0.f, 0.f};
    const char* ar = lx + (lane & 7) * 160 + (lane >> 4) * 8;
    const char* br = lw + ((lane >> 4) * 4 + ((lane & 15) >> 2)) * QSTR + (lane & 3) * 8;
#pragma unroll
    for (int s = 0; s < 5; ++s) {
      const s16x4 a = *reinterpret_cast<const s16x4*>(ar + s * 32);
#pragma unroll
      for (int t = 0; t < 4; ++t) qa[t] = __builtin_amdgcn_mfma_f32_16x16x16bf16_1k(a, tr_read(br + s * 16 * QSTR + t * 32), qa[t], 0, 0, 0);
    }
    const u64 T = E + 1, tag = T & TAG_MASK;
    gu64* gq = ws + WS_GQ + ((h * B + ((lane >> 4) & 1) * 4) * 8 + b) * DH + wave * 64 + (lane >> 5) * 16 + (lane & 15);
#pragma unroll
    for (int t = 0; t < 4; t += 2)
#pragma unroll
      for (int r = 0; r < 4; ++r) {
        auto s = __builtin_amdgcn_permlane32_swap(__float_as_uint(qa[t][r]), __float_as_uint(qa[t + 1][r]), false, false);
        __hip_atomic_store(gq + r * 8 * DH + t * 16, gran24(__uint_as_float(s[0]), tag), RLX_AGENT);
      }
    STAMP(8);
    {
      unsigned oa = (unsigned)(WS_GQ + ((h * B + b) * 8 + (lane >> 5)) * DH + wave * 64 + (lane & 31) * 2) * 8;
      u32x4 gr[4];
      for (unsigned spins = 0;; ++spins) {
        asm volatile("" : "+v"(oa));
#pragma unroll
        for (int i = 0; i < 4; ++i) gr[i] = __builtin_amdgcn_raw_buffer_load_b128(rsW, oa, 2 * i * DH * 8, 16);
        bool ok = true;
#pragma unroll
        for (int i = 0; i < 4; ++i) ok &= tag40_ok(gr[i][0], gr[i][1], tag) && tag40_ok(gr[i][2], gr[i][3], tag);
        if (__all(ok) || spins > SPIN_CAP) break;
      }
      float a0 = 0.f, a1 = 0.f;
#pragma unroll
      for (int i = 0; i < 4; ++i) { a0 += pay24(gr[i][1]); a1 += pay24(gr[i][3]); }
      a0 = swapadd32(a0, a0); a1 = swapadd32(a1, a1);
      if (lane < 32) reinterpret_cast<unsigned*>(lds + L_Q)[wave * 32 + lane] = cvtpk(a0, a1);
      if (tid == 0) { *reinterpret_cast<u64*>(lds + L_T) = T; *reinterpret_cast<unsigned*>(lds + L_T + 8) = 0; *reinterpret_cast<unsigned*>(lds + L_T + 12) = (unsigned)t_in; }
      STAMP(9);
    }
  }
  else asm volatile("s_barrier" ::: "memory");
  SB();

  const unsigned kvoff = g * 384 + c * 16;
  const int rot = (bid * ROT_MUL) & 63;
#define GOFF(n) ((unsigned)((((n) + rot) & 63) * 8 + wave) * 3072u)
  u32x4 buf[24];
#pragma unroll
  for (int u = 0; u < 8; ++u) {
#pragma unroll
    for (int t = 0; t < 3; ++t) buf[u * 3 + t] = bload(rsK, kvoff + t * 128, GOFF(u), 1);
    SB();
    if (INF < 8 && u >= INF - 1 && u < 7 && wave >= 3) asm volatile("s_waitcnt vmcnt(%0)" :: "n"(3 * (INF - 1)) : "memory");
    SB();
  }
  STAMP(10);
  wg_barrier();
  if (tid == NTHR - 64) __hip_atomic_store(ws + WS_SEEN + bid, *reinterpret_cast<const u64*>(lds + L_T), RLX_AGENT);
  u32x4 qv[3];
#pragma unroll
  for (int t = 0; t < 3; ++t) qv[t] = *reinterpret_cast<const u32x4*>(lds + L_Q + t * 128 + c * 16);
  STAMP(1);

  const unsigned t0 = __builtin_amdgcn_readfirstlane(*reinterpret_cast<const unsigned*>(lds + L_T + 12));
  auto pace = [&]() {
    unsigned k = 0;
    if (lane == 0) k = __hip_atomic_fetch_add(reinterpret_cast<unsigned*>(lds + L_T + 8), 1u, __ATOMIC_RELAXED, __HIP_MEMORY_SCOPE_WORKGROUP);
    k = __builtin_amdgcn_readfirstlane(k);
    const unsigned due = ((k + 8 - PACE_C) * (PACE_T * 2)) >> 8;
    for (int spins = 0; spins < 4096 && (unsigned)__builtin_amdgcn_s_memrealtime() - t0 < due; ++spins) __builtin_amdgcn_s_sleep(2);
    SB();
  };

  float* pw = reinterpret_cast<float*>(lds + L_P) + wave * 512;
  auto kbatch = [&](int bt, auto last) {
    pace();
    float xs[8];
#pragma unroll
    for (int u = 0; u < 8; ++u) {
      float d = 0.f;
#pragma unroll
      for (int t = 0; t < 3; ++t)
#pragma unroll
        for (int k = 0; k < 4; ++k) d = dot2(buf[u * 3 + t][k], qv[t][k], d);
      PIN1(d);
      xs[u] = d;
      SB();
#pragma unroll
      for (int t = 0; t < 3; ++t)
        buf[u * 3 + t] = last ? bload(rsV, kvoff + t * 128, GOFF(u), 1) : bload(rsK, kvoff + t * 128, GOFF(bt * 8 + 8 + u), 1);
      SB();
    }
    float y[4], z[2];
#pragma unroll
    for (int j = 0; j < 4; ++j) { const float t1 = xs[j] + dpp<HMIR>(xs[j]), t2 = xs[j + 4] + dpp<HMIR>(xs[j + 4]); y[j] = (c & 4) ? t2 : t1; }
#pragma unroll
    for (int j = 0; j < 2; ++j) { const float t1 = y[j] + dpp<XOR2>(y[j]), t2 = y[j + 2] + dpp<XOR2>(y[j + 2]); z[j] = (c & 2) ? t2 : t1; }
    const float t1 = z[0] + dpp<XOR1>(z[0]), t2 = z[1] + dpp<XOR1>(z[1]);
    pw[bt * 64 + lane] = (c & 1) ? t2 : t1;
  };
  for (int bt = 0; bt < 7; ++bt) kbatch(bt, std::false_type{});
  u32x4 wv[5];
  {
    const __amdgpu_buffer_rsrc_t rsO = __builtin_amdgcn_make_buffer_rsrc((void*)(wo + (size_t)h * DH * D * 2), 0, DH * D * 2, 0x00020000);
#pragma unroll
    for (int i = 0; i < 5; ++i) {
      const int cid = i * 64 + lane;
      wv[i] = __builtin_amdgcn_raw_buffer_load_b128(rsO, (unsigned)(wave * 32 + cid / 10) * (D * 2) + b * 160 + (cid % 10) * 16, 0, W_AUX);
    }
    SB();
  }
  kbatch(7, std::true_type{});
#pragma unroll
  for (int i = 0; i < 5; ++i) {
    const int cid = i * 64 + lane;
    *reinterpret_cast<u32x4*>(lds + L_WO + (wave * 32 + cid / 10) * OSTR + (cid % 10) * 16) = wv[i];
  }
  STAMP(2);

  float m, l;
  {
    float s[8];
    LDS_FENCE();
#pragma unroll
    for (int i = 0; i < 8; ++i) s[i] = pw[i * 64 + lane];
    m = s[0];
#pragma unroll
    for (int i = 1; i < 8; ++i) m = fmaxf(m, s[i]);
    m = wave_max(m);
    l = 0.f;
#pragma unroll
    for (int i = 0; i < 8; ++i) { const float p = __builtin_amdgcn_exp2f((s[i] - m) * C2); l += p; pw[i * 64 + lane] = p; }
    l = wave_sum(l);
    LDS_FENCE();
  }

  float acc[24];
#pragma unroll
  for (int j = 0; j < 24; ++j) acc[j] = 0.f;
  auto vbatch = [&](int bt, auto last) {
    if (!last) pace();
    const f32x4 p0 = *reinterpret_cast<const f32x4*>(pw + bt * 64 + g * 8), p1 = *reinterpret_cast<const f32x4*>(pw + bt * 64 + g * 8 + 4);
#pragma unroll
    for (int u = 0; u < 8; ++u) {
      fma24(acc, &buf[u * 3], u < 4 ? p0[u & 3] : p1[u & 3]);
      PIN24(acc);
      SB();
      if (!last) {
#pragma unroll
        for (int t = 0; t < 3; ++t) buf[u * 3 + t] = bload(rsV, kvoff + t * 128, GOFF(bt * 8 + 8 + u), 1);
        SB();
      }
    }
  };
  for (int bt = 0; bt < 7; ++bt) vbatch(bt, std::false_type{});
  vbatch(7, std::true_type{});
  STAMP(3);

  {
    float r[3]; reduce24(acc, r, lane);
    float* red = reinterpret_cast<float*>(lds + L_RED) + wave * 192;
#pragma unroll
    for (int a = 0; a < 3; ++a) { const int A = 3 * g + a; red[(A >> 3) * 64 + c * 8 + (A & 7)] = r[a]; }
    if (lane == 0) *reinterpret_cast<f32x2*>(lds + L_ML + wave * 8) = f32x2{m, l};
  }
  wg_barrier();
  const u64 T = *reinterpret_cast<const u64*>(lds + L_T);
  const u64 tag = T & TAG_MASK;
  if (tid < 96) {
    const float* ml = reinterpret_cast<const float*>(lds + L_ML);
    float M = ml[0];
#pragma unroll
    for (int w = 1; w < NW; ++w) M = fmaxf(M, ml[2 * w]);
    float L = 0.f, s0 = 0.f, s1 = 0.f;
#pragma unroll
    for (int w = 0; w < NW; ++w) {
      const float f = __builtin_amdgcn_exp2f((ml[2 * w] - M) * C2);
      const f32x2 v = *reinterpret_cast<const f32x2*>(lds + L_RED + w * 768 + tid * 8);
      L = fmaf(f, ml[2 * w + 1], L);
      s0 = fmaf(f, v[0], s0); s1 = fmaf(f, v[1], s1);
    }
    __hip_atomic_store(ws + WS_GO + (h * B + b) * 96 + tid, ((u64)cvtpk(s0 / L, s1 / L) << 32) | (unsigned)T, RLX_AGENT);
  }
  STAMP(4);
  if (wave < 6) {
    unsigned ga = (unsigned)(WS_GO + h * B * 96 + wave * 128 + lane * 2) * 8;
    u32x4 v;
    for (unsigned spins = 0;; ++spins) {
      asm volatile("" : "+v"(ga));
      v = __builtin_amdgcn_raw_buffer_load_b128(rsW, ga, 0, 16);
      if (__all(v[0] == (unsigned)T && v[2] == (unsigned)T) || spins > SPIN_CAP) break;
      __builtin_amdgcn_s_sleep(POLL_SLEEP);
    }
    const int n = wave * 128 + lane * 2, bb = n / 96;
    *reinterpret_cast<u32x2*>(lds + L_OALL + bb * ASTR + (n - bb * 96) * 4) = u32x2{v[1], v[3]};
  }
  wg_barrier();
  STAMP(11);

  if (wave < 5) {
    f32x4 oa = {0.f, 0.f, 0.f, 0.f};
    const char* ar = lds + L_OALL + (lane & 7) * ASTR + (lane >> 4) * 16;
    const char* br = lds + L_WO + ((lane >> 4) * 8 + ((lane & 15) >> 2)) * OSTR + wave * 32 + (lane & 3) * 8;
#pragma unroll
    for (int s = 0; s < 6; ++s) {
      const bf16x8 a = *reinterpret_cast<const bf16x8*>(ar + s * 64);
      const s16x4 b0 = tr_read(br + s * 32 * OSTR), b1 = tr_read(br + (s * 32 + 4) * OSTR);
      const bf16x8 bb = __builtin_bit_cast(bf16x8, __builtin_shufflevector(b0, b1, 0, 1, 2, 3, 4, 5, 6, 7));
      oa = __builtin_amdgcn_mfma_f32_16x16x32_bf16(a, bb, oa, 0, 0, 0);
    }
    const int col = wave * 16 + (lane & 15), qq = col / 20, cr = col - qq * 20;
#pragma unroll
    for (int s = 0; s < 2; ++s) {
      auto sw = __builtin_amdgcn_permlane32_swap(__float_as_uint(oa[s]), __float_as_uint(oa[s + 2]), false, false);
      const int bb = ((lane >> 4) & 1) * 4 + s + 2 * (lane >> 5);
      __hip_atomic_store(ws + WS_GF + ((b * 32 + bb * 4 + qq) * 32 + h) * 20 + cr, gran24(__uint_as_float(sw[0]), tag), RLX_AGENT);
    }
    STAMP(5);
    gu64* gp = ws + WS_GF + (b * 32 + h) * 32 * 20 + (lane >> 2) * 20 + wave * 4 + (lane & 3);
    u64 g0, g1;
    for (unsigned spins = 0;; ++spins) {
      g0 = __hip_atomic_load(gp, RLX_AGENT);
      g1 = __hip_atomic_load(gp + 16 * 20, RLX_AGENT);
      const bool ok = (g0 & TAG_MASK) == tag && (g1 & TAG_MASK) == tag;
      if (__all(ok) || spins > SPIN_CAP) break;
      __builtin_amdgcn_s_sleep(POLL_SLEEP);
    }
    float v = __uint_as_float((unsigned)(g0 >> 40) << 8) + __uint_as_float((unsigned)(g1 >> 40) << 8);
    v += dpp<ROR4>(v); v += dpp<ROR8>(v);
    v = swapadd16(v, v);
    v = swapadd32(v, v);
    if (lane < 4) out[(h >> 2) * D + b * 80 + (h & 3) * 20 + wave * 4 + lane] = (u16)cvtpk(v, v);
  } else if (wave == 7 && bid == 0) {
    for (unsigned spins = 0;; ++spins) {
      bool ok = true;
#pragma unroll
      for (int i = 0; i < 4; ++i) ok &= __hip_atomic_load(ws + WS_SEEN + i * 64 + lane, RLX_AGENT) == T;
      if (__all(ok) || spins > SPIN_CAP) break;
    }
    if (lane == 0) __hip_atomic_store(ws + WS_EPOCH, T, RLX_AGENT);
  }
  STAMP(6);
}
}

extern "C" void kernel_launch(void* const* d_in, const int* in_sizes, int n_in, void* d_out, int out_size, void* d_ws, size_t ws_size,
                              hipStream_t stream) {
  attn_fused<<<256, NTHR, 0, stream>>>((const u16*)d_in[0], (const char*)d_in[1], (const char*)d_in[2], (const char*)d_in[3],
                                       (const char*)d_in[4], (u16*)d_out, (u64*)d_ws);
}
```

```cpp
#include <hip/hip_runtime.h>
#include <hip/hip_bf16.h>
#include <stdint.h>

namespace {
constexpr int B = 8, D = 640, H = 32, SK = 4096, DH = 192, N = H * DH;
constexpr int NW = 8, NTHR = NW * 64;
constexpr unsigned HEADB = SK * DH * 2;
constexpr float C2 = 0.07216878364870322f * 1.4426950408889634f;

typedef unsigned short u16;
typedef unsigned long long u64;
using u32x4 = __attribute__((ext_vector_type(4))) unsigned;
using f32x4 = __attribute__((ext_vector_type(4))) float;
using u32x2 = __attribute__((ext_vector_type(2))) unsigned;
using f32x2 = __attribute__((ext_vector_type(2))) float;
using bf16x2 = __attribute__((ext_vector_type(2))) __bf16;
using bf16x8 = __attribute__((ext_vector_type(8))) __bf16;
using s16x4 = __attribute__((ext_vector_type(4))) short;
typedef __attribute__((address_space(3))) s16x4* lds_s16x4;
typedef __attribute__((address_space(1))) u64 gu64;
#define RLX_AGENT __ATOMIC_RELAXED, __HIP_MEMORY_SCOPE_AGENT

constexpr int WS_EPOCH = 0, WS_SEEN = 64, WS_GQ = 1024, WS_GO = WS_GQ + H * B * 8 * DH, WS_GF = WS_GO + H * B * 96;
constexpr int WS_STAMP = 1 << 20;
constexpr u64 TAG_MASK = (1ull << 40) - 1;
constexpr unsigned SPIN_CAP = 1u << 20;
#define W_AUX 2
#define X_AUX 0
#define ROT_MUL 7
#define PRO_BAR 1
#define INF 2
#define PACE_T 11900
#define PACE_C 1
#define POLL_SLEEP 1
__device__ __forceinline__ float lo16(unsigned w) { return __builtin_bit_cast(float, w << 16); }
__device__ __forceinline__ float hi16(unsigned w) { return __builtin_bit_cast(float, w & 0xffff0000u); }
__device__ __forceinline__ unsigned cvtpk(float a, float b) {
  unsigned r; asm("v_cvt_pk_bf16_f32 %0, %1, %2" : "=v"(r) : "v"(a), "v"(b)); return r;
}
__device__ __forceinline__ float dot2(unsigned k, unsigned q, float acc) {
  return __builtin_amdgcn_fdot2_f32_bf16(__builtin_bit_cast(bf16x2, k), __builtin_bit_cast(bf16x2, q), acc, false);
}
template <int CTRL> __device__ __forceinline__ float dpp(float x) {
  return __builtin_bit_cast(float, __builtin_amdgcn_mov_dpp(__builtin_bit_cast(int, x), CTRL, 0xf, 0xf, true));
}
constexpr int XOR1 = 0xB1, XOR2 = 0x4E, HMIR = 0x141, ROR8 = 0x128, ROR4 = 0x124;
__device__ __forceinline__ float swapadd32(float x, float y) {
  auto s = __builtin_amdgcn_permlane32_swap(__float_as_uint(x), __float_as_uint(y), false, false);
  return __uint_as_float(s[0]) + __uint_as_float(s[1]);
}
__device__ __forceinline__ float swapadd16(float x, float y) {
  auto s = __builtin_amdgcn_permlane16_swap(__float_as_uint(x), __float_as_uint(y), false, false);
  return __uint_as_float(s[0]) + __uint_as_float(s[1]);
}
__device__ __forceinline__ float wave_max(float x) {
  x = fmaxf(x, dpp<XOR1>(x)); x = fmaxf(x, dpp<XOR2>(x)); x = fmaxf(x, dpp<HMIR>(x)); x = fmaxf(x, dpp<ROR8>(x));
  auto s = __builtin_amdgcn_permlane16_swap(__float_as_uint(x), __float_as_uint(x), false, false);
  x = fmaxf(__uint_as_float(s[0]), __uint_as_float(s[1]));
  auto t = __builtin_amdgcn_permlane32_swap(__float_as_uint(x), __float_as_uint(x), false, false);
  return fmaxf(__uint_as_float(t[0]), __uint_as_float(t[1]));
}
__device__ __forceinline__ float wave_sum(float x) {
  x += dpp<XOR1>(x); x += dpp<XOR2>(x); x += dpp<HMIR>(x); x += dpp<ROR8>(x);
  x = swapadd16(x, x);
  return swapadd32(x, x);
}
__device__ __forceinline__ void reduce24(const float (&acc)[24], float (&r)[3], int lane) {
  float r1[12], r2[6];
#pragma unroll
  for (int a = 0; a < 12; ++a) r1[a] = swapadd32(acc[a], acc[a + 12]);
#pragma unroll
  for (int a = 0; a < 6; ++a) r2[a] = swapadd16(r1[a], r1[a + 6]);
#pragma unroll
  for (int a = 0; a < 3; ++a) { const float t1 = r2[a] + dpp<ROR8>(r2[a]), t2 = r2[a + 3] + dpp<ROR8>(r2[a + 3]); r[a] = (lane & 8) ? t2 : t1; }
}
__device__ __forceinline__ u32x4 bload(__amdgpu_buffer_rsrc_t rs, unsigned voff, unsigned soff, int aux_nt) {
  return aux_nt ? __builtin_amdgcn_raw_buffer_load_b128(rs, voff, soff, 2) : __builtin_amdgcn_raw_buffer_load_b128(rs, voff, soff, 0);
}
__device__ __forceinline__ void fma24(float (&acc)[24], const u32x4* w, float p) {
#pragma unroll
  for (int i = 0; i < 3; ++i)
#pragma unroll
    for (int k = 0; k < 4; ++k) {
      acc[i * 8 + 2 * k] = fmaf(p, lo16(w[i][k]), acc[i * 8 + 2 * k]);
      acc[i * 8 + 2 * k + 1] = fmaf(p, hi16(w[i][k]), acc[i * 8 + 2 * k + 1]);
    }
}
#define SB() __builtin_amdgcn_sched_barrier(0)
#define PIN1(a) asm volatile("" : "+v"(a) :: "memory")
#define PIN8(a, o) asm volatile("" : "+v"(a[o]), "+v"(a[o + 1]), "+v"(a[o + 2]), "+v"(a[o + 3]), "+v"(a[o + 4]), "+v"(a[o + 5]), "+v"(a[o + 6]), "+v"(a[o + 7]) :: "memory")
#define PIN24(a) do { PIN8(a, 0); PIN8(a, 8); PIN8(a, 16); } while (0)
#define LDS_FENCE() asm volatile("s_waitcnt lgkmcnt(0)" ::: "memory")
__device__ __forceinline__ void wg_barrier() {
  asm volatile("s_waitcnt lgkmcnt(0)\n\ts_barrier" ::: "memory");
}
#define STAMP(i) do { } while (0)

constexpr int QSTR = 144, OSTR = 176, ASTR = 416;
constexpr int L_P = 0;
constexpr int L_RED = L_P + NW * 512 * 4;
constexpr int L_ML = L_RED + NW * 192 * 4;
constexpr int L_Q = L_ML + 2 * NW * 4;
constexpr int L_T = L_Q + 384;
constexpr int L_OALL = L_T + 16;
constexpr int L_X = L_OALL + 8 * ASTR;
constexpr int L_WQ = L_X + 3 * 1280;
constexpr int L_WO = L_WQ + 3 * 80 * QSTR;
constexpr int L_END = L_WO + 256 * OSTR;

__device__ __forceinline__ s16x4 tr_read(const char* p) { return __builtin_amdgcn_ds_read_tr16_b64_v4i16((lds_s16x4)p); }
__device__ __forceinline__ u64 gran24(float v, u64 tag) { return ((u64)((__float_as_uint(v) + 0x80u) >> 8) << 40) | tag; }
__device__ __forceinline__ float pay24(unsigned hi) { return __uint_as_float(hi & 0xffffff00u); }
__device__ __forceinline__ bool tag40_ok(unsigned lo, unsigned hi, u64 tag) { return lo == (unsigned)tag && (hi & 0xffu) == (unsigned)(tag >> 32); }

__global__ void __launch_bounds__(NTHR, 2)
attn_fused(const u16* __restrict__ x, const char* __restrict__ kc, const char* __restrict__ vc, const char* __restrict__ wq,
           const char* __restrict__ wo, u16* __restrict__ out, u64* __restrict__ ws_) {
  __shared__ __attribute__((aligned(16))) char lds[L_END];
  gu64* ws = (gu64*)ws_;
  const int tid = threadIdx.x, lane = tid & 63, wave = __builtin_amdgcn_readfirstlane(tid >> 6);
  const int bid = blockIdx.x, h = (bid & 7) * 4 + ((bid >> 3) & 3), b = bid >> 5, bh = b * H + h;
  const int g = lane >> 3, c = lane & 7;
  STAMP(0);
  const u64 t_in = __builtin_amdgcn_s_memrealtime();
  const __amdgpu_buffer_rsrc_t rsK = __builtin_amdgcn_make_buffer_rsrc((void*)(kc + (size_t)bh * HEADB), 0, HEADB, 0x00020000);
  const __amdgpu_buffer_rsrc_t rsV = __builtin_amdgcn_make_buffer_rsrc((void*)(vc + (size_t)bh * HEADB), 0, HEADB, 0x00020000);
  const __amdgpu_buffer_rsrc_t rsW = __builtin_amdgcn_make_buffer_rsrc((void*)ws_, 0, (WS_GF + 8 * 32 * H * 20) * 8, 0x00020000);

  if (wave < 3) {
    const __amdgpu_buffer_rsrc_t rsQ = __builtin_amdgcn_make_buffer_rsrc((void*)(wq + h * (DH * 2)), 0, D * N * 2 - h * (DH * 2), 0x00020000);
    const __amdgpu_buffer_rsrc_t rsX = __builtin_amdgcn_make_buffer_rsrc((void*)x, 0, B * D * 2, 0x00020000);
    const u64 E = __hip_atomic_load(ws + WS_EPOCH, RLX_AGENT);
    const int c1 = 64 + (lane & 15);
    const u32x4 x0 = __builtin_amdgcn_raw_buffer_load_b128(rsX, (lane / 10) * (D * 2) + b * 160 + (lane % 10) * 16, 0, X_AUX);
    const u32x4 x1 = __builtin_amdgcn_raw_buffer_load_b128(rsX, (c1 / 10) * (D * 2) + b * 160 + (c1 % 10) * 16, 0, X_AUX);
    u32x4 wt[10];
    const unsigned voff = (unsigned)(b * 80 + g) * (N * 2) + wave * 128 + c * 16;
#pragma unroll
    for (int i = 0; i < 10; ++i) wt[i] = __builtin_amdgcn_raw_buffer_load_b128(rsQ, voff, i * 8 * (N * 2), W_AUX);
    SB();
    asm volatile("s_barrier" ::: "memory");
    STAMP(7);
    char* lx = lds + L_X + wave * 1280;
    char* lw = lds + L_WQ + wave * (80 * QSTR);
    *reinterpret_cast<u32x4*>(lx + lane * 16) = x0;
    *reinterpret_cast<u32x4*>(lx + c1 * 16) = x1;
#pragma unroll
    for (int i = 0; i < 10; ++i) *reinterpret_cast<u32x4*>(lw + (i * 8 + g) * QSTR + c * 16) = wt[i];
    f32x4 qa[4];
#pragma unroll
    for (int t = 0; t < 4; ++t) qa[t] = f32x4{0.f, 0.f, 0.f, 0.f};
    const char* ar = lx + (lane & 7) * 160 + (lane >> 4) * 8;
    const char* br = lw + ((lane >> 4) * 4 + ((lane & 15) >> 2)) * QSTR + (lane & 3) * 8;
#pragma unroll
    for (int s = 0; s < 5; ++s) {
      const s16x4 a = *reinterpret_cast<const s16x4*>(ar + s * 32);
#pragma unroll
      for (int t = 0; t < 4; ++t) qa[t] = __builtin_amdgcn_mfma_f32_16x16x16bf16_1k(a, tr_read(br + s * 16 * QSTR + t * 32), qa[t], 0, 0, 0);
    }
    const u64 T = E + 1, tag = T & TAG_MASK;
    gu64* gq = ws + WS_GQ + ((h * B + ((lane >> 4) & 1) * 4) * 8 + b) * DH + wave * 64 + (lane >> 5) * 16 + (lane & 15);
#pragma unroll
    for (int t = 0; t < 4; t += 2)
#pragma unroll
      for (int r = 0; r < 4; ++r) {
        auto s = __builtin_amdgcn_permlane32_swap(__float_as_uint(qa[t][r]), __float_as_uint(qa[t + 1][r]), false, false);
        __hip_atomic_store(gq + r * 8 * DH + t * 16, gran24(__uint_as_float(s[0]), tag), RLX_AGENT);
      }
    STAMP(8);
    {
      unsigned oa = (unsigned)(WS_GQ + ((h * B + b) * 8 + (lane >> 5)) * DH + wave * 64 + (lane & 31) * 2) * 8;
      u32x4 gr[4];
      for (unsigned spins = 0;; ++spins) {
        asm volatile("" : "+v"(oa));
#pragma unroll
        for (int i = 0; i < 4; ++i) gr[i] = __builtin_amdgcn_raw_buffer_load_b128(rsW, oa, 2 * i * DH * 8, 16);
        bool ok = true;
#pragma unroll
        for (int i = 0; i < 4; ++i) ok &= tag40_ok(gr[i][0], gr[i][1], tag) && tag40_ok(gr[i][2], gr[i][3], tag);
        if (__all(ok) || spins > SPIN_CAP) break;
      }
      float a0 = 0.f, a1 = 0.f;
#pragma unroll
      for (int i = 0; i < 4; ++i) { a0 += pay24(gr[i][1]); a1 += pay24(gr[i][3]); }
      a0 = swapadd32(a0, a0); a1 = swapadd32(a1, a1);
      if (lane < 32) reinterpret_cast<unsigned*>(lds + L_Q)[wave * 32 + lane] = cvtpk(a0, a1);
      if (tid == 0) { *reinterpret_cast<u64*>(lds + L_T) = T; *reinterpret_cast<unsigned*>(lds + L_T + 8) = 0; *reinterpret_cast<unsigned*>(lds + L_T + 12) = (unsigned)t_in; }
      STAMP(9);
    }
  }
  else asm volatile("s_barrier" ::: "memory");
  SB();

  const unsigned kvoff = g * 384 + c * 16;
  const int rot = (bid * ROT_MUL) & 63;
#define GOFF(n) ((unsigned)((((n) + rot) & 63) * 8 + wave) * 3072u)
  u32x4 buf[24];
#pragma unroll
  for (int u = 0; u < 8; ++u) {
#pragma unroll
    for (int t = 0; t < 3; ++t) buf[u * 3 + t] = bload(rsK, kvoff + t * 128, GOFF(u), 1);
    SB();
    if (INF < 8 && u >= INF - 1 && u < 7 && wave >= 3) asm volatile("s_waitcnt vmcnt(%0)" :: "n"(3 * (INF - 1)) : "memory");
    SB();
  }
  STAMP(10);
  wg_barrier();
  if (tid == NTHR - 64) {
    const u64 TT = *reinterpret_cast<const u64*>(lds + L_T);
    __hip_atomic_store(ws + WS_SEEN + 256 + bid, (TT << 32) | *reinterpret_cast<const unsigned*>(lds + L_T + 12), RLX_AGENT);
    __hip_atomic_store(ws + WS_SEEN + bid, TT, RLX_AGENT);
  }
  u32x4 qv[3];
#pragma unroll
  for (int t = 0; t < 3; ++t) qv[t] = *reinterpret_cast<const u32x4*>(lds + L_Q + t * 128 + c * 16);
  STAMP(1);

  const unsigned t0 = __builtin_amdgcn_readfirstlane(*reinterpret_cast<const unsigned*>(lds + L_T + 12));
  auto pace = [&]() {
    unsigned k = 0;
    if (lane == 0) k = __hip_atomic_fetch_add(reinterpret_cast<unsigned*>(lds + L_T + 8), 1u, __ATOMIC_RELAXED, __HIP_MEMORY_SCOPE_WORKGROUP);
    k = __builtin_amdgcn_readfirstlane(k);
    const unsigned due = ((k + 8 - PACE_C) * (PACE_T * 2)) >> 8;
    for (int spins = 0; spins < 4096 && (unsigned)__builtin_amdgcn_s_memrealtime() - t0 < due; ++spins) __builtin_amdgcn_s_sleep(2);
    SB();
  };

  float* pw = reinterpret_cast<float*>(lds + L_P) + wave * 512;
  auto kbatch = [&](int bt, auto last) {
    pace();
    float xs[8];
#pragma unroll
    for (int u = 0; u < 8; ++u) {
      float d = 0.f;
#pragma unroll
      for (int t = 0; t < 3; ++t)
#pragma unroll
        for (int k = 0; k < 4; ++k) d = dot2(buf[u * 3 + t][k], qv[t][k], d);
      PIN1(d);
      xs[u] = d;
      SB();
#pragma unroll
      for (int t = 0; t < 3; ++t)
        buf[u * 3 + t] = last ? bload(rsV, kvoff + t * 128, GOFF(u), 1) : bload(rsK, kvoff + t * 128, GOFF(bt * 8 + 8 + u), 1);
      SB();
    }
    float y[4], z[2];
#pragma unroll
    for (int j = 0; j < 4; ++j) { const float t1 = xs[j] + dpp<HMIR>(xs[j]), t2 = xs[j + 4] + dpp<HMIR>(xs[j + 4]); y[j] = (c & 4) ? t2 : t1; }
#pragma unroll
    for (int j = 0; j < 2; ++j) { const float t1 = y[j] + dpp<XOR2>(y[j]), t2 = y[j + 2] + dpp<XOR2>(y[j + 2]); z[j] = (c & 2) ? t2 : t1; }
    const float t1 = z[0] + dpp<XOR1>(z[0]), t2 = z[1] + dpp<XOR1>(z[1]);
    pw[bt * 64 + lane] = (c & 1) ? t2 : t1;
  };
  for (int bt = 0; bt < 7; ++bt) kbatch(bt, std::false_type{});
  u32x4 wv[5];
  {
    const __amdgpu_buffer_rsrc_t rsO = __builtin_amdgcn_make_buffer_rsrc((void*)(wo + (size_t)h * DH * D * 2), 0, DH * D * 2, 0x00020000);
#pragma unroll
    for (int i = 0; i < 5; ++i) {
      const int cid = i * 64 + lane;
      wv[i] = __builtin_amdgcn_raw_buffer_load_b128(rsO, (unsigned)(wave * 32 + cid / 10) * (D * 2) + b * 160 + (cid % 10) * 16, 0, W_AUX);
    }
    SB();
  }
  kbatch(7, std::true_type{});
#pragma unroll
  for (int i = 0; i < 5; ++i) {
    const int cid = i * 64 + lane;
    *reinterpret_cast<u32x4*>(lds + L_WO + (wave * 32 + cid / 10) * OSTR + (cid % 10) * 16) = wv[i];
  }
  STAMP(2);

  float m, l;
  {
    float s[8];
    LDS_FENCE();
#pragma unroll
    for (int i = 0; i < 8; ++i) s[i] = pw[i * 64 + lane];
    m = s[0];
#pragma unroll
    for (int i = 1; i < 8; ++i) m = fmaxf(m, s[i]);
    m = wave_max(m);
    l = 0.f;
#pragma unroll
    for (int i = 0; i < 8; ++i) { const float p = __builtin_amdgcn_exp2f((s[i] - m) * C2); l += p; pw[i * 64 + lane] = p; }
    l = wave_sum(l);
    LDS_FENCE();
  }

  float acc[24];
#pragma unroll
  for (int j = 0; j < 24; ++j) acc[j] = 0.f;
  auto vbatch = [&](int bt, auto last) {
    if (!last) pace();
    const f32x4 p0 = *reinterpret_cast<const f32x4*>(pw + bt * 64 + g * 8), p1 = *reinterpret_cast<const f32x4*>(pw + bt * 64 + g * 8 + 4);
#pragma unroll
    for (int u = 0; u < 8; ++u) {
      fma24(acc, &buf[u * 3], u < 4 ? p0[u & 3] : p1[u & 3]);
      PIN24(acc);
      SB();
      if (!last) {
#pragma unroll
        for (int t = 0; t < 3; ++t) buf[u * 3 + t] = bload(rsV, kvoff + t * 128, GOFF(bt * 8 + 8 + u), 1);
        SB();
      }
    }
  };
  for (int bt = 0; bt < 7; ++bt) vbatch(bt, std::false_type{});
  vbatch(7, std::true_type{});
  STAMP(3);

  {
    float r[3]; reduce24(acc, r, lane);
    float* red = reinterpret_cast<float*>(lds + L_RED) + wave * 192;
#pragma unroll
    for (int a = 0; a < 3; ++a) { const int A = 3 * g + a; red[(A >> 3) * 64 + c * 8 + (A & 7)] = r[a]; }
    if (lane == 0) *reinterpret_cast<f32x2*>(lds + L_ML + wave * 8) = f32x2{m, l};
  }
  wg_barrier();
  const u64 T = *reinterpret_cast<const u64*>(lds + L_T);
  const u64 tag = T & TAG_MASK;
  if (tid < 96) {
    const float* ml = reinterpret_cast<const float*>(lds + L_ML);
    float M = ml[0];
#pragma unroll
    for (int w = 1; w < NW; ++w) M = fmaxf(M, ml[2 * w]);
    float L = 0.f, s0 = 0.f, s1 = 0.f;
#pragma unroll
    for (int w = 0; w < NW; ++w) {
      const float f = __builtin_amdgcn_exp2f((ml[2 * w] - M) * C2);
      const f32x2 v = *reinterpret_cast<const f32x2*>(lds + L_RED + w * 768 + tid * 8);
      L = fmaf(f, ml[2 * w + 1], L);
      s0 = fmaf(f, v[0], s0); s1 = fmaf(f, v[1], s1);
    }
    __hip_atomic_store(ws + WS_GO + (h * B + b) * 96 + tid, ((u64)cvtpk(s0 / L, s1 / L) << 32) | (unsigned)T, RLX_AGENT);
  }
  STAMP(4);
  if (wave < 6) {
    unsigned ga = (unsigned)(WS_GO + h * B * 96 + wave * 128 + lane * 2) * 8;
    u32x4 v;
    for (unsigned spins = 0;; ++spins) {
      asm volatile("" : "+v"(ga));
      v = __builtin_amdgcn_raw_buffer_load_b128(rsW, ga, 0, 16);
      if (__all(v[0] == (unsigned)T && v[2] == (unsigned)T) || spins > SPIN_CAP) break;
      __builtin_amdgcn_s_sleep(POLL_SLEEP);
    }
    const int n = wave * 128 + lane * 2, bb = n / 96;
    *reinterpret_cast<u32x2*>(lds + L_OALL + bb * ASTR + (n - bb * 96) * 4) = u32x2{v[1], v[3]};
  }
  wg_barrier();
  STAMP(11);

  if (wave < 5) {
    f32x4 oa = {0.f, 0.f, 0.f, 0.f};
    const char* ar = lds + L_OALL + (lane & 7) * ASTR + (lane >> 4) * 16;
    const char* br = lds + L_WO + ((lane >> 4) * 8 + ((lane & 15) >> 2)) * OSTR + wave * 32 + (lane & 3) * 8;
#pragma unroll
    for (int s = 0; s < 6; ++s) {
      const bf16x8 a = *reinterpret_cast<const bf16x8*>(ar + s * 64);
      const s16x4 b0 = tr_read(br + s * 32 * OSTR), b1 = tr_read(br + (s * 32 + 4) * OSTR);
      const bf16x8 bb = __builtin_bit_cast(bf16x8, __builtin_shufflevector(b0, b1, 0, 1, 2, 3, 4, 5, 6, 7));
      oa = __builtin_amdgcn_mfma_f32_16x16x32_bf16(a, bb, oa, 0, 0, 0);
    }
    const int col = wave * 16 + (lane & 15), qq = col / 20, cr = col - qq * 20;
#pragma unroll
    for (int s = 0; s < 2; ++s) {
      auto sw = __builtin_amdgcn_permlane32_swap(__float_as_uint(oa[s]), __float_as_uint(oa[s + 2]), false, false);
      const int bb = ((lane >> 4) & 1) * 4 + s + 2 * (lane >> 5);
      __hip_atomic_store(ws + WS_GF + ((b * 32 + bb * 4 + qq) * 32 + h) * 20 + cr, gran24(__uint_as_float(sw[0]), tag), RLX_AGENT);
    }
    STAMP(5);
    gu64* gp = ws + WS_GF + (b * 32 + h) * 32 * 20 + (lane >> 2) * 20 + wave * 4 + (lane & 3);
    u64 g0, g1;
    for (unsigned spins = 0;; ++spins) {
      g0 = __hip_atomic_load(gp, RLX_AGENT);
      g1 = __hip_atomic_load(gp + 16 * 20, RLX_AGENT);
      const bool ok = (g0 & TAG_MASK) == tag && (g1 & TAG_MASK) == tag;
      if (__all(ok) || spins > SPIN_CAP) break;
      __builtin_amdgcn_s_sleep(POLL_SLEEP);
    }
    float v = __uint_as_float((unsigned)(g0 >> 40) << 8) + __uint_as_float((unsigned)(g1 >> 40) << 8);
    v += dpp<ROR4>(v); v += dpp<ROR8>(v);
    v = swapadd16(v, v);
    v = swapadd32(v, v);
    if (lane < 4) out[(h >> 2) * D + b * 80 + (h & 3) * 20 + wave * 4 + lane] = (u16)cvtpk(v, v);
  } else if (wave == 7 && bid == 0) {
    for (unsigned spins = 0;; ++spins) {
      bool ok = true;
#pragma unroll
      for (int i = 0; i < 4; ++i) ok &= __hip_atomic_load(ws + WS_SEEN + i * 64 + lane, RLX_AGENT) == T;
      if (__all(ok) || spins > SPIN_CAP) break;
    }
    unsigned mn = 0xffffffffu, mx = 0, ref = 0;
    {
      u64 w[4];
      for (unsigned spins = 0;; ++spins) {
        bool ok = true;
        for (int i = 0; i < 4; ++i) { w[i] = __hip_atomic_load(ws + WS_SEEN + 256 + i * 64 + lane, RLX_AGENT); ok &= (unsigned)(w[i] >> 32) == (unsigned)T; }
        if (__all(ok) || spins > SPIN_CAP) break;
      }
      ref = __builtin_amdgcn_readfirstlane((unsigned)w[0]) - 100000u;
      for (int i = 0; i < 4; ++i) { const unsigned d = (unsigned)w[i] - ref; mn = min(mn, d); mx = max(mx, d); }
      for (int o = 1; o < 64; o <<= 1) { mn = min(mn, (unsigned)__shfl_xor((int)mn, o)); mx = max(mx, (unsigned)__shfl_xor((int)mx, o)); }
    }
    if (lane == 0) __hip_atomic_store(ws + WS_EPOCH, T, RLX_AGENT);
    const unsigned until = mn + 20000u;
    for (int spins = 0; spins < 100000 && (unsigned)__builtin_amdgcn_s_memrealtime() - ref < until; ++spins) __builtin_amdgcn_s_sleep(1);
  }
  STAMP(6);
}
}

extern "C" void kernel_launch(void* const* d_in, const int* in_sizes, int n_in, void* d_out, int out_size, void* d_ws, size_t ws_size,
                              hipStream_t stream) {
  attn_fused<<<256, NTHR, 0, stream>>>((const u16*)d_in[0], (const char*)d_in[1], (const char*)d_in[2], (const char*)d_in[3],
                                       (const char*)d_in[4], (u16*)d_out, (u64*)d_ws);
}
```

```cpp
#include <hip/hip_runtime.h>
#include <hip/hip_bf16.h>
#include <stdint.h>

namespace {
constexpr int B = 8, D = 640, H = 32, SK = 4096, DH = 192, N = H * DH;
constexpr int NW = 8, NTHR = NW * 64;
constexpr unsigned HEADB = SK * DH * 2;
constexpr float C2 = 0.07216878364870322f * 1.4426950408889634f;

typedef unsigned short u16;
typedef unsigned long long u64;
using u32x4 = __attribute__((ext_vector_type(4))) unsigned;
using f32x4 = __attribute__((ext_vector_type(4))) float;
using u32x2 = __attribute__((ext_vector_type(2))) unsigned;
using f32x2 = __attribute__((ext_vector_type(2))) float;
using bf16x2 = __attribute__((ext_vector_type(2))) __bf16;
using bf16x8 = __attribute__((ext_vector_type(8))) __bf16;
using s16x4 = __attribute__((ext_vector_type(4))) short;
typedef __attribute__((address_space(3))) s16x4* lds_s16x4;
typedef __attribute__((address_space(1))) u64 gu64;
#define RLX_AGENT __ATOMIC_RELAXED, __HIP_MEMORY_SCOPE_AGENT


constexpr int WS_EPOCH = 0, WS_SEEN = 64, WS_T0 = 512, WS_GQ = 1024, WS_GO = WS_GQ + H * B * 8 * DH, WS_GF = WS_GO + H * B * 96;
constexpr u64 TAG_MASK = (1ull << 40) - 1;
constexpr unsigned SPIN_CAP = 1u << 20;
#define W_AUX 2
#define X_AUX 0
#define ROT_MUL 7
#define ODD_DT (-100)
#define PACE_T 11400
#define PACE_O 60
#define PG 2
#define PH_MUL 1
#define ANCHOR_CAP 800
#define POLL_SLEEP 1
__device__ __forceinline__ unsigned cvtpk(float a, float b) {
  unsigned r; asm("v_cvt_pk_bf16_f32 %0, %1, %2" : "=v"(r) : "v"(a), "v"(b)); return r;
}
__device__ __forceinline__ float dot2(unsigned k, unsigned q, float acc) {
  return __builtin_amdgcn_fdot2_f32_bf16(__builtin_bit_cast(bf16x2, k), __builtin_bit_cast(bf16x2, q), acc, false);
}
template <int CTRL> __device__ __forceinline__ float dpp(float x) {
  return __builtin_bit_cast(float, __builtin_amdgcn_mov_dpp(__builtin_bit_cast(int, x), CTRL, 0xf, 0xf, true));
}
constexpr int XOR1 = 0xB1, XOR2 = 0x4E, HMIR = 0x141, ROR8 = 0x128, ROR4 = 0x124;
__device__ __forceinline__ float swapadd32(float x, float y) {
  auto s = __builtin_amdgcn_permlane32_swap(__float_as_uint(x), __float_as_uint(y), false, false);
  return __uint_as_float(s[0]) + __uint_as_float(s[1]);
}
__device__ __forceinline__ float swapadd16(float x, float y) {
  auto s = __builtin_amdgcn_permlane16_swap(__float_as_uint(x), __float_as_uint(y), false, false);
  return __uint_as_float(s[0]) + __uint_as_float(s[1]);
}
__device__ __forceinline__ float wave_max(float x) {
  x = fmaxf(x, dpp<XOR1>(x)); x = fmaxf(x, dpp<XOR2>(x)); x = fmaxf(x, dpp<HMIR>(x)); x = fmaxf(x, dpp<ROR8>(x));
  auto s = __builtin_amdgcn_permlane16_swap(__float_as_uint(x), __float_as_uint(x), false, false);
  x = fmaxf(__uint_as_float(s[0]), __uint_as_float(s[1]));
  auto t = __builtin_amdgcn_permlane32_swap(__float_as_uint(x), __float_as_uint(x), false, false);
  return fmaxf(__uint_as_float(t[0]), __uint_as_float(t[1]));
}
__device__ __forceinline__ float wave_sum(float x) {
  x += dpp<XOR1>(x); x += dpp<XOR2>(x); x += dpp<HMIR>(x); x += dpp<ROR8>(x);
  x = swapadd16(x, x);
  return swapadd32(x, x);
}
__device__ __forceinline__ void reduce24(const float (&acc)[24], float (&r)[3], int lane) {
  float r1[12], r2[6];
#pragma unroll
  for (int a = 0; a < 12; ++a) r1[a] = swapadd32(acc[a], acc[a + 12]);
#pragma unroll
  for (int a = 0; a < 6; ++a) r2[a] = swapadd16(r1[a], r1[a + 6]);
#pragma unroll
  for (int a = 0; a < 3; ++a) { const float t1 = r2[a] + dpp<ROR8>(r2[a]), t2 = r2[a + 3] + dpp<ROR8>(r2[a + 3]); r[a] = (lane & 8) ? t2 : t1; }
}
__device__ __forceinline__ u32x4 bload(__amdgpu_buffer_rsrc_t rs, unsigned voff, unsigned soff, int aux_nt) {
  return aux_nt ? __builtin_amdgcn_raw_buffer_load_b128(rs, voff, soff, 2) : __builtin_amdgcn_raw_buffer_load_b128(rs, voff, soff, 0);
}
__device__ __forceinline__ void dot24(float (&acc)[24], const u32x4* w, unsigned pl) {
  const unsigned ph = pl << 16;
#pragma unroll
  for (int i = 0; i < 3; ++i)
#pragma unroll
    for (int k = 0; k < 4; ++k) {
      acc[i * 8 + 2 * k] = dot2(w[i][k], pl, acc[i * 8 + 2 * k]);
      acc[i * 8 + 2 * k + 1] = dot2(w[i][k], ph, acc[i * 8 + 2 * k + 1]);
    }
}
#define SB() __builtin_amdgcn_sched_barrier(0)
__device__ __forceinline__ u32x2 poll1(__amdgpu_buffer_rsrc_t rs, unsigned off, unsigned tlo, unsigned thi, unsigned msk) {
  u32x4 v;
  for (unsigned spins = 0;; ++spins) {
    asm volatile("" : "+v"(off));
    v = __builtin_amdgcn_raw_buffer_load_b128(rs, off, 0, 16);
    const bool ok = v[0] == tlo && v[2] == tlo && ((v[1] ^ thi) & msk) == 0 && ((v[3] ^ thi) & msk) == 0;
    if (__all(ok) || spins > SPIN_CAP) break;
    __builtin_amdgcn_s_sleep(POLL_SLEEP);
  }
  return u32x2{v[1], v[3]};
}
#define PIN1(a) asm volatile("" : "+v"(a) :: "memory")
#define PIN8(a, o) asm volatile("" : "+v"(a[o]), "+v"(a[o + 1]), "+v"(a[o + 2]), "+v"(a[o + 3]), "+v"(a[o + 4]), "+v"(a[o + 5]), "+v"(a[o + 6]), "+v"(a[o + 7]) :: "memory")
#define PIN24(a) do { PIN8(a, 0); PIN8(a, 8); PIN8(a, 16); } while (0)
#define LDS_FENCE() asm volatile("s_waitcnt lgkmcnt(0)" ::: "memory")
__device__ __forceinline__ void wg_barrier() {
  asm volatile("s_waitcnt lgkmcnt(0)\n\ts_barrier" ::: "memory");
}


constexpr int QSTR = 144, OSTR = 176, ASTR = 416;
constexpr int L_P = 0;
constexpr int L_RED = L_P + NW * 512 * 4;
constexpr int L_ML = L_RED + NW * 192 * 4;
constexpr int L_Q = L_ML + 2 * NW * 4;
constexpr int L_T = L_Q + 384;
constexpr int L_OALL = L_T + 16;
constexpr int L_X = L_OALL + 8 * ASTR;
constexpr int L_WQ = L_X + 3 * 1280;
constexpr int L_WO = L_WQ + 3 * 80 * QSTR;
constexpr int L_END = L_WO + 256 * OSTR;

__device__ __forceinline__ s16x4 tr_read(const char* p) { return __builtin_amdgcn_ds_read_tr16_b64_v4i16((lds_s16x4)p); }
__device__ __forceinline__ u64 gran24(float v, u64 tag) { return ((u64)((__float_as_uint(v) + 0x80u) >> 8) << 40) | tag; }
__device__ __forceinline__ float pay24(unsigned hi) { return __uint_as_float(hi & 0xffffff00u); }
__device__ __forceinline__ bool tag40_ok(unsigned lo, unsigned hi, u64 tag) { return lo == (unsigned)tag && (hi & 0xffu) == (unsigned)(tag >> 32); }

__global__ void __launch_bounds__(NTHR, 2)
attn_fused(const u16* __restrict__ x, const char* __restrict__ kc, const char* __restrict__ vc, const char* __restrict__ wq,
           const char* __restrict__ wo, u16* __restrict__ out, u64* __restrict__ ws_) {
  __shared__ __attribute__((aligned(16))) char lds[L_END];
  gu64* ws = (gu64*)ws_;
  const int tid = threadIdx.x, lane = tid & 63, wave = __builtin_amdgcn_readfirstlane(tid >> 6);
  const int bid = blockIdx.x, h = (bid & 7) * 4 + ((bid >> 3) & 3), b = bid >> 5, bh = b * H + h;
  const int g = lane >> 3, c = lane & 7;
  const u64 t_in = __builtin_amdgcn_s_memrealtime();
  const __amdgpu_buffer_rsrc_t rsK = __builtin_amdgcn_make_buffer_rsrc((void*)(kc + (size_t)bh * HEADB), 0, HEADB, 0x00020000);
  const __amdgpu_buffer_rsrc_t rsV = __builtin_amdgcn_make_buffer_rsrc((void*)(vc + (size_t)bh * HEADB), 0, HEADB, 0x00020000);
  const __amdgpu_buffer_rsrc_t rsW = __builtin_amdgcn_make_buffer_rsrc((void*)ws_, 0, (WS_GF + 8 * 32 * H * 20) * 8, 0x00020000);

  u64 T1 = 0;
  if (wave < 3) {
    const __amdgpu_buffer_rsrc_t rsQ = __builtin_amdgcn_make_buffer_rsrc((void*)(wq + h * (DH * 2)), 0, D * N * 2 - h * (DH * 2), 0x00020000);
    const __amdgpu_buffer_rsrc_t rsX = __builtin_amdgcn_make_buffer_rsrc((void*)x, 0, B * D * 2, 0x00020000);
    if (tid == 0) { *reinterpret_cast<unsigned*>(lds + L_T + 8) = 64; *reinterpret_cast<unsigned*>(lds + L_T + 12) = (unsigned)t_in; }
    const u64 E = __hip_atomic_load(ws + WS_EPOCH, RLX_AGENT);
    const int c1 = 64 + (lane & 15);
    const u32x4 x0 = __builtin_amdgcn_raw_buffer_load_b128(rsX, (lane / 10) * (D * 2) + b * 160 + (lane % 10) * 16, 0, X_AUX);
    const u32x4 x1 = __builtin_amdgcn_raw_buffer_load_b128(rsX, (c1 / 10) * (D * 2) + b * 160 + (c1 % 10) * 16, 0, X_AUX);
    u32x4 wt[10];
    const unsigned voff = (unsigned)(b * 80 + g) * (N * 2) + wave * 128 + c * 16;
#pragma unroll
    for (int i = 0; i < 10; ++i) wt[i] = __builtin_amdgcn_raw_buffer_load_b128(rsQ, voff, i * 8 * (N * 2), W_AUX);
    SB();
    wg_barrier();
    if (tid == 0) __hip_atomic_store(ws + WS_T0 + bid, (t_in << 32) | (unsigned)(E + 1), RLX_AGENT);
    char* lx = lds + L_X + wave * 1280;
    char* lw = lds + L_WQ + wave * (80 * QSTR);
    *reinterpret_cast<u32x4*>(lx + lane * 16) = x0;
    *reinterpret_cast<u32x4*>(lx + c1 * 16) = x1;
#pragma unroll
    for (int i = 0; i < 10; ++i) *reinterpret_cast<u32x4*>(lw + (i * 8 + g) * QSTR + c * 16) = wt[i];
    f32x4 qa[4];
#pragma unroll
    for (int t = 0; t < 4; ++t) qa[t] = f32x4{0.f, 0.f, 0.f, 0.f};
    const char* ar = lx + (lane & 7) * 160 + (lane >> 4) * 8;
    const char* br = lw + ((lane >> 4) * 4 + ((lane & 15) >> 2)) * QSTR + (lane & 3) * 8;
#pragma unroll
    for (int s = 0; s < 5; ++s) {
      const s16x4 a = *reinterpret_cast<const s16x4*>(ar + s * 32);
#pragma unroll
      for (int t = 0; t < 4; ++t) qa[t] = __builtin_amdgcn_mfma_f32_16x16x16bf16_1k(a, tr_read(br + s * 16 * QSTR + t * 32), qa[t], 0, 0, 0);
    }
    T1 = E + 1;
    const u64 tag = T1 & TAG_MASK;
    gu64* gq = ws + WS_GQ + ((h * B + ((lane >> 4) & 1) * 4) * 8 + b) * DH + wave * 64 + (lane >> 5) * 16 + (lane & 15);
#pragma unroll
    for (int t = 0; t < 4; t += 2)
#pragma unroll
      for (int r = 0; r < 4; ++r) {
        auto s = __builtin_amdgcn_permlane32_swap(__float_as_uint(qa[t][r]), __float_as_uint(qa[t + 1][r]), false, false);
        __hip_atomic_store(gq + r * 8 * DH + t * 16, gran24(__uint_as_float(s[0]), tag), RLX_AGENT);
      }
  }
  else wg_barrier();
  SB();

  auto pace = [&](unsigned wgt) {
    unsigned k = 0, t0 = 0;
    if (lane == 0) {
      k = __hip_atomic_fetch_add(reinterpret_cast<unsigned*>(lds + L_T + 8), wgt, __ATOMIC_RELAXED, __HIP_MEMORY_SCOPE_WORKGROUP);
      t0 = __hip_atomic_load(reinterpret_cast<unsigned*>(lds + L_T + 12), __ATOMIC_RELAXED, __HIP_MEMORY_SCOPE_WORKGROUP);
    }
    k = __builtin_amdgcn_readfirstlane(k);
    t0 = __builtin_amdgcn_readfirstlane(t0);
    const unsigned kk = k & ~(8u * PG - 1);
    const int due = PACE_O + (int)((kk * (PACE_T - ODD_DT) + ((bid & 1) ? 0 : 2 * ODD_DT * (int)min(kk, 512u))) >> 10) + (int)((unsigned)(((bid >> 3) * PH_MUL) & 31) * (PG - 1) * (8 * PACE_T >> 10) / 31);
    for (int spins = 0; spins < 4096 && (int)((unsigned)__builtin_amdgcn_s_memrealtime() - t0) < due; ++spins) __builtin_amdgcn_s_sleep(2);
    SB();
  };

  const unsigned kvoff = g * 384 + c * 16;
  const int rot = (bid * ROT_MUL) & 63;
#define GOFF(n) ((unsigned)((((n) >> 3) * 8 + wave + rot) & 63) * 24576u + (unsigned)((n) & 7) * 3072u)
  u32x4 buf[24];
  if (wave >= 3) {
    const unsigned t0 = __builtin_amdgcn_readfirstlane(*reinterpret_cast<const unsigned*>(lds + L_T + 12));
    const int due = PACE_O + (int)(((wave < 5 ? wave - 3 : wave) * 8 * PACE_T) >> 10);
    for (int spins = 0; spins < 4096 && (int)((unsigned)__builtin_amdgcn_s_memrealtime() - t0) < due; ++spins) __builtin_amdgcn_s_sleep(2);
  }
  SB();
#pragma unroll
  for (int u = 0; u < 8; ++u) {
#pragma unroll
    for (int t = 0; t < 3; ++t) buf[u * 3 + t] = bload(rsK, kvoff + t * 128, GOFF(u), 1);
    SB();
  }
  if (wave < 3) {
      const u64 T = T1, tag = T & TAG_MASK;
      unsigned oa = (unsigned)(WS_GQ + ((h * B + b) * 8 + (lane >> 5)) * DH + wave * 64 + (lane & 31) * 2) * 8;
      u32x4 gr[4];
      unsigned ot = WS_T0 * 8 + lane * 16;
      u32x4 gt = {0, 0, 0, 0};
      for (unsigned spins = 0;; ++spins) {
        asm volatile("" : "+v"(oa));
        asm volatile("" : "+v"(ot));
        if (wave == 0) gt = __builtin_amdgcn_raw_buffer_load_b128(rsW, ot, 0, 16);
#pragma unroll
        for (int i = 0; i < 4; ++i) gr[i] = __builtin_amdgcn_raw_buffer_load_b128(rsW, oa, 2 * i * DH * 8, 16);
        bool ok = true;
#pragma unroll
        for (int i = 0; i < 4; ++i) ok &= tag40_ok(gr[i][0], gr[i][1], tag) && tag40_ok(gr[i][2], gr[i][3], tag);
        if (__all(ok) || spins > SPIN_CAP) break;
      }
      float a0 = 0.f, a1 = 0.f;
#pragma unroll
      for (int i = 0; i < 4; ++i) { a0 += pay24(gr[i][1]); a1 += pay24(gr[i][3]); }
      a0 = swapadd32(a0, a0); a1 = swapadd32(a1, a1);
      if (lane < 32) reinterpret_cast<unsigned*>(lds + L_Q)[wave * 32 + lane] = cvtpk(a0, a1);
      if (tid == 0) *reinterpret_cast<u64*>(lds + L_T) = T;
      if (wave == 0) {
        const unsigned to = (unsigned)t_in;
        const int dn = (int)((unsigned)__builtin_amdgcn_s_memrealtime() - to) - 150;
        const int d0 = gt[0] == (unsigned)T ? (int)(gt[1] - to) : dn, d1 = gt[2] == (unsigned)T ? (int)(gt[3] - to) : dn;
        const float dm = wave_sum((float)(max(-ANCHOR_CAP, min(ANCHOR_CAP, d0)) + max(-ANCHOR_CAP, min(ANCHOR_CAP, d1)))) * (1.f / 128.f);
        if (lane == 0) __hip_atomic_store(reinterpret_cast<unsigned*>(lds + L_T + 12), to + (unsigned)(int)dm, __ATOMIC_RELAXED, __HIP_MEMORY_SCOPE_WORKGROUP);
      }
    }
  wg_barrier();
  if (tid == NTHR - 64) __hip_atomic_store(ws + WS_SEEN + bid, *reinterpret_cast<const u64*>(lds + L_T), RLX_AGENT);
  u32x4 qv[3];
#pragma unroll
  for (int t = 0; t < 3; ++t) qv[t] = *reinterpret_cast<const u32x4*>(lds + L_Q + t * 128 + c * 16);

  float* pw = reinterpret_cast<float*>(lds + L_P) + wave * 512;
  auto kbatch = [&](int bt, auto last) {
    pace(8);
    float xs[8];
#pragma unroll
    for (int u = 0; u < 8; ++u) {
      float d = 0.f;
#pragma unroll
      for (int t = 0; t < 3; ++t)
#pragma unroll
        for (int k = 0; k < 4; ++k) d = dot2(buf[u * 3 + t][k], qv[t][k], d);
      PIN1(d);
      xs[u] = d;
      SB();
#pragma unroll
      for (int t = 0; t < 3; ++t)
        buf[u * 3 + t] = last ? bload(rsV, kvoff + t * 128, GOFF(u), 1) : bload(rsK, kvoff + t * 128, GOFF(bt * 8 + 8 + u), 1);
      SB();
    }
    float y[4], z[2];
#pragma unroll
    for (int j = 0; j < 4; ++j) { const float t1 = xs[j] + dpp<HMIR>(xs[j]), t2 = xs[j + 4] + dpp<HMIR>(xs[j + 4]); y[j] = (c & 4) ? t2 : t1; }
#pragma unroll
    for (int j = 0; j < 2; ++j) { const float t1 = y[j] + dpp<XOR2>(y[j]), t2 = y[j + 2] + dpp<XOR2>(y[j + 2]); z[j] = (c & 2) ? t2 : t1; }
    const float t1 = z[0] + dpp<XOR1>(z[0]), t2 = z[1] + dpp<XOR1>(z[1]);
    pw[bt * 64 + lane] = (c & 1) ? t2 : t1;
  };
  for (int bt = 0; bt < 7; ++bt) kbatch(bt, std::false_type{});
  u32x4 wv[5];
  {
    const __amdgpu_buffer_rsrc_t rsO = __builtin_amdgcn_make_buffer_rsrc((void*)(wo + (size_t)h * DH * D * 2), 0, DH * D * 2, 0x00020000);
#pragma unroll
    for (int i = 0; i < 5; ++i) {
      const int cid = i * 64 + lane;
      wv[i] = __builtin_amdgcn_raw_buffer_load_b128(rsO, (unsigned)(wave * 32 + cid / 10) * (D * 2) + b * 160 + (cid % 10) * 16, 0, W_AUX);
    }
    SB();
  }
  kbatch(7, std::true_type{});
#pragma unroll
  for (int i = 0; i < 5; ++i) {
    const int cid = i * 64 + lane;
    *reinterpret_cast<u32x4*>(lds + L_WO + (wave * 32 + cid / 10) * OSTR + (cid % 10) * 16) = wv[i];
  }

  float m, l;
  {
    float s[8];
    LDS_FENCE();
#pragma unroll
    for (int i = 0; i < 8; ++i) s[i] = pw[i * 64 + lane];
    m = s[0];
#pragma unroll
    for (int i = 1; i < 8; ++i) m = fmaxf(m, s[i]);
    m = wave_max(m);
    l = 0.f;
#pragma unroll
    for (int i = 0; i < 8; ++i) {
      const float p = __builtin_amdgcn_exp2f((s[i] - m) * C2);
      const unsigned pb = cvtpk(p, 0.f);
      l += __uint_as_float(pb << 16); pw[i * 64 + lane] = __uint_as_float(pb);
    }
    l = wave_sum(l);
    LDS_FENCE();
  }

  float acc[24];
#pragma unroll
  for (int j = 0; j < 24; ++j) acc[j] = 0.f;
  auto vbatch = [&](int bt, auto last) {
    if (!last) pace(8);
    const f32x4 p0 = *reinterpret_cast<const f32x4*>(pw + bt * 64 + g * 8), p1 = *reinterpret_cast<const f32x4*>(pw + bt * 64 + g * 8 + 4);
#pragma unroll
    for (int u = 0; u < 8; ++u) {
      dot24(acc, &buf[u * 3], __float_as_uint(u < 4 ? p0[u & 3] : p1[u & 3]));
      PIN24(acc);
      SB();
      if (!last) {
#pragma unroll
        for (int t = 0; t < 3; ++t) buf[u * 3 + t] = bload(rsV, kvoff + t * 128, GOFF(bt * 8 + 8 + u), 1);
        SB();
      }
    }
  };
  for (int bt = 0; bt < 7; ++bt) vbatch(bt, std::false_type{});
  vbatch(7, std::true_type{});

  {
    float r[3]; reduce24(acc, r, lane);
    float* red = reinterpret_cast<float*>(lds + L_RED) + wave * 192;
#pragma unroll
    for (int a = 0; a < 3; ++a) { const int A = 3 * g + a; red[(A >> 3) * 64 + c * 8 + (A & 7)] = r[a]; }
    if (lane == 0) *reinterpret_cast<f32x2*>(lds + L_ML + wave * 8) = f32x2{m, l};
  }
  wg_barrier();
  const u64 T = *reinterpret_cast<const u64*>(lds + L_T);
  const u64 tag = T & TAG_MASK;
  if (tid < 96) {
    const float* ml = reinterpret_cast<const float*>(lds + L_ML);
    float M = ml[0];
#pragma unroll
    for (int w = 1; w < NW; ++w) M = fmaxf(M, ml[2 * w]);
    float L = 0.f, s0 = 0.f, s1 = 0.f;
#pragma unroll
    for (int w = 0; w < NW; ++w) {
      const float f = __builtin_amdgcn_exp2f((ml[2 * w] - M) * C2);
      const f32x2 v = *reinterpret_cast<const f32x2*>(lds + L_RED + w * 768 + tid * 8);
      L = fmaf(f, ml[2 * w + 1], L);
      s0 = fmaf(f, v[0], s0); s1 = fmaf(f, v[1], s1);
    }
    __hip_atomic_store(ws + WS_GO + (h * B + b) * 96 + tid, ((u64)cvtpk(s0 / L, s1 / L) << 32) | (unsigned)T, RLX_AGENT);
  }
  const unsigned tlo = __builtin_amdgcn_readfirstlane((unsigned)T), thi = __builtin_amdgcn_readfirstlane((unsigned)(T >> 32));
  if (wave < 6) {
    const unsigned go = (unsigned)(WS_GO + h * B * 96 + wave * 128 + lane * 2) * 8;
    const u32x2 v = poll1(rsW, go, tlo, 0u, 0u);
    const int n = wave * 128 + lane * 2, bb = n / 96;
    *reinterpret_cast<u32x2*>(lds + L_OALL + bb * ASTR + (n - bb * 96) * 4) = v;
  }
  wg_barrier();

  if (wave < 5) {
    f32x4 oa = {0.f, 0.f, 0.f, 0.f};
    const char* ar = lds + L_OALL + (lane & 7) * ASTR + (lane >> 4) * 16;
    const char* br = lds + L_WO + ((lane >> 4) * 8 + ((lane & 15) >> 2)) * OSTR + wave * 32 + (lane & 3) * 8;
#pragma unroll
    for (int s = 0; s < 6; ++s) {
      const bf16x8 a = *reinterpret_cast<const bf16x8*>(ar + s * 64);
      const s16x4 b0 = tr_read(br + s * 32 * OSTR), b1 = tr_read(br + (s * 32 + 4) * OSTR);
      const bf16x8 bb = __builtin_bit_cast(bf16x8, __builtin_shufflevector(b0, b1, 0, 1, 2, 3, 4, 5, 6, 7));
      oa = __builtin_amdgcn_mfma_f32_16x16x32_bf16(a, bb, oa, 0, 0, 0);
    }
    const int col = wave * 16 + (lane & 15), qq = col / 20, cr = col - qq * 20;
#pragma unroll
    for (int s = 0; s < 2; ++s) {
      auto sw = __builtin_amdgcn_permlane32_swap(__float_as_uint(oa[s]), __float_as_uint(oa[s + 2]), false, false);
      const int bb = ((lane >> 4) & 1) * 4 + s + 2 * (lane >> 5);
      __hip_atomic_store(ws + WS_GF + (b * 32 + bb * 4 + qq) * 640 + (cr >> 2) * 128 + h * 4 + (cr & 3), gran24(__uint_as_float(sw[0]), tag), RLX_AGENT);
    }
    const int bo = (h >> 2) * D + b * 80 + (h & 3) * 20 + wave * 4 + lane * 2;
    const unsigned gf = (unsigned)(WS_GF + (b * 32 + h) * 640 + wave * 128 + lane * 2) * 8;
    const u32x2 r = poll1(rsW, gf, tlo, thi, 0xffu);
    float v0 = pay24(r[0]), v1 = pay24(r[1]);
    v0 += dpp<XOR2>(v0); v1 += dpp<XOR2>(v1);
    v0 += dpp<ROR4>(v0); v1 += dpp<ROR4>(v1);
    v0 += dpp<ROR8>(v0); v1 += dpp<ROR8>(v1);
    v0 = swapadd16(v0, v0); v1 = swapadd16(v1, v1);
    v0 = swapadd32(v0, v0); v1 = swapadd32(v1, v1);
    if (lane < 2) *reinterpret_cast<unsigned*>(out + bo) = cvtpk(v0, v1);
  } else if (wave == 7 && bid == 0) {
    for (unsigned spins = 0;; ++spins) {
      bool ok = true;
#pragma unroll
      for (int i = 0; i < 4; ++i) ok &= __hip_atomic_load(ws + WS_SEEN + i * 64 + lane, RLX_AGENT) == T;
      if (__all(ok) || spins > SPIN_CAP) break;
    }
    if (lane == 0) __hip_atomic_store(ws + WS_EPOCH, T, RLX_AGENT);
  }
}
}

extern "C" void kernel_launch(void* const* d_in, const int* in_sizes, int n_in, void* d_out, int out_size, void* d_ws, size_t ws_size,
                              hipStream_t stream) {
  attn_fused<<<256, NTHR, 0, stream>>>((const u16*)d_in[0], (const char*)d_in[1], (const char*)d_in[2], (const char*)d_in[3],
                                       (const char*)d_in[4], (u16*)d_out, (u64*)d_ws);
}
```
